```python
import torch

def _softmax(x, axis=-1):
    return torch.softmax(x, dim=axis)
def _gelu(x):
    return torch.nn.functional.gelu(x, approximate="tanh")
def _silu(x):
    return torch.nn.functional.silu(x)
def _sigmoid(x):
    return torch.sigmoid(x)
def _layernorm(x, axis=-1, eps=1e-5):
    m = x.mean(dim=axis, keepdim=True)
    v = x.var(dim=axis, keepdim=True, unbiased=False)
    return (x - m) / torch.sqrt(v + eps)
def _scatter_add(tgt, idx, src, axis=0):
    return tgt.clone().index_add_(axis, idx, src)
def _segment_sum(src, seg, n):
    out = torch.zeros((n,) + src.shape[1:], dtype=src.dtype)
    return out.index_add_(0, seg, src)
def _pad(x, pw):
    flat = tuple(v for pair in reversed(pw) for v in pair)
    return torch.nn.functional.pad(x, flat)

kernel_name = 'interleave_layers__mha_block__glu_gated_reglu__prompt'

def setup_inputs():

    _w = (lambda a: (lambda t: t.to(torch.bfloat16) if t.is_floating_point() else t)(torch.as_tensor(a)))
    def _A_setup():
        B, S, D, H = 1, 2048, 640, 32
        return {
            "x": _w((lambda *s: torch.randn(*s, dtype=torch.bfloat16) * 0.05)(B, S, D)),
            "wqkv": _w((lambda *s: torch.randn(*s, dtype=torch.bfloat16) * 0.05)(D, 3 * D)),
            "wo": _w((lambda *s: torch.randn(*s, dtype=torch.bfloat16) * 0.05)(D, D)),
        }
    _A_in = _A_setup()

    def _B_setup():
        B, S, D, F = 1, 2048, 640, 1536
        return {
            "x": _w((lambda *s: torch.randn(*s, dtype=torch.bfloat16) * 0.05)(B, S, D)),
            "W_gate": _w((lambda *s: torch.randn(*s, dtype=torch.bfloat16) * 0.8)(D, F) * (2.0 / D) ** 0.5),
            "W_up": _w((lambda *s: torch.randn(*s, dtype=torch.bfloat16) * 0.8)(D, F) * (2.0 / D) ** 0.5),
            "W_down": _w((lambda *s: torch.randn(*s, dtype=torch.bfloat16) * 0.8)(F, D) * (1.0 / F) ** 0.5),
        }
    _B_in = _B_setup()

    return {
        "A_x": _A_in["x"],
        "A_wqkv": _A_in["wqkv"],
        "A_wo": _A_in["wo"],
        "B_x": _B_in["x"],
        "B_W_gate": _B_in["W_gate"],
        "B_W_up": _B_in["W_up"],
        "B_W_down": _B_in["W_down"],
    }

def reference(A_x, A_wqkv, A_wo, B_x, B_W_gate, B_W_up, B_W_down):
    def _A_ref(x, wqkv, wo):
        B, S, D = x.shape
        H = 32
        Dh = D // H
        qkv = (lambda x, *d: x.permute(*d))((x @ wqkv).reshape(B, S, 3, H, Dh), 2, 0, 3, 1, 4)
        q, k, v = qkv[0], qkv[1], qkv[2]
        scores = torch.einsum("bhqd,bhkd->bhqk", q, k) * (Dh ** -0.5)
        p = _softmax(scores, axis=-1)
        o = torch.einsum("bhqk,bhkd->bhqd", p, v)
        return (lambda x, *d: x.permute(*d))(o, 0, 2, 1, 3).reshape(B, S, D) @ wo

    def _B_ref(x, W_gate, W_up, W_down):
        gate = torch.einsum("bsd,df->bsf", x, W_gate)
        up = torch.einsum("bsd,df->bsf", x, W_up)
        gate = gate * (lambda x, dt: x.to(dt))(gate > 0, gate.dtype)
        h = gate * up
        return torch.einsum("bsf,fd->bsd", h, W_down)

    x = A_x
    x = x + _A_ref(_layernorm(x, axis=-1), A_wqkv, A_wo)
    x = x + _B_ref(_layernorm(x, axis=-1), B_W_gate, B_W_up, B_W_down)
    x = x + _A_ref(_layernorm(x, axis=-1), A_wqkv, A_wo)
    x = x + _B_ref(_layernorm(x, axis=-1), B_W_gate, B_W_up, B_W_down)
    return x
```

```cpp
#include <hip/hip_runtime.h>
#include <hip/hip_bf16.h>

typedef short bf16x8 __attribute__((ext_vector_type(8)));
typedef short bf16x4 __attribute__((ext_vector_type(4)));
typedef float f32x16 __attribute__((ext_vector_type(16)));
typedef float f32x4 __attribute__((ext_vector_type(4)));
typedef float f32x2 __attribute__((ext_vector_type(2)));
typedef unsigned u32x4 __attribute__((ext_vector_type(4)));
typedef unsigned u32x2 __attribute__((ext_vector_type(2)));
typedef __bf16 bf2_t __attribute__((ext_vector_type(2)));
#define LAS __attribute__((address_space(3)))
#define DI __device__ __forceinline__
#define MFMA32(a, b, c) __builtin_amdgcn_mfma_f32_32x32x16_bf16((a), (b), (c), 0, 0, 0)
#define MFMA32K8(a, b, c) __builtin_amdgcn_mfma_f32_32x32x8bf16_1k((a), (b), (c), 0, 0, 0)
#define MFMA16(a, b, c) __builtin_amdgcn_mfma_f32_16x16x32_bf16((a), (b), (c), 0, 0, 0)
#define WAITV(n) asm volatile("s_waitcnt vmcnt(%0)" ::"i"(n) : "memory")
#define BAR_LGKM() asm volatile("s_waitcnt lgkmcnt(0)\n\ts_barrier" ::: "memory")
#define BAR_RAW() asm volatile("s_barrier" ::: "memory")

constexpr int SEQ = 2048, DM = 640, NH = 32, DH = 20, FF = 1536;
constexpr int KVCH = 21504;
constexpr int KV_K1 = 8192, KV_V = 10240, VSTEP = 672;
constexpr float QSCALE = 0.22360679774997896f * 1.4426950408889634f;
constexpr float LN_EPS = 1e-5f;

constexpr size_t WS_X1 = 0, WS_X2 = WS_X1 + (size_t)SEQ * DM * 2, WS_X3 = WS_X2 + (size_t)SEQ * DM * 2;
constexpr size_t WS_Q = WS_X3 + (size_t)SEQ * DM * 2, WS_O = WS_Q + (size_t)SEQ * DM * 2;
constexpr size_t WS_KV = WS_O + (size_t)SEQ * DM * 2, WS_H = WS_KV + (size_t)NH * 8 * KVCH;
constexpr size_t WS_ST = WS_H + (size_t)SEQ * FF * 2;
constexpr size_t WS_CSQ = WS_ST + (size_t)SEQ * 8 * 8;
constexpr size_t WS_CSG = WS_CSQ + 2 * 1920 * 4, WS_CSU = WS_CSG + 2 * FF * 4;

DI unsigned pack2(float a, float b) { f32x2 v = {a, b}; return __builtin_bit_cast(unsigned, __builtin_convertvector(v, bf2_t)); }
DI float bflo(unsigned u) { return __uint_as_float(u << 16); }
DI float bfhi(unsigned u) { return __uint_as_float(u & 0xffff0000u); }
DI void glds16(const char* g, LAS char* l) { __builtin_amdgcn_global_load_lds((const __attribute__((address_space(1))) void*)g, (LAS void*)l, 16, 0, 0); }
DI bf16x8 lds128(LAS const char* p) { return *(LAS const bf16x8*)p; }
DI bf16x4 lds64(LAS const char* p) { return *(LAS const bf16x4*)p; }
DI bf16x4 ldstr(LAS const char* p) { return __builtin_bit_cast(bf16x4, __builtin_amdgcn_ds_read_tr16_b64_v4i16((LAS bf16x4*)p)); }
DI bf16x8 cat(bf16x4 a, bf16x4 b) { return __builtin_shufflevector(a, b, 0, 1, 2, 3, 4, 5, 6, 7); }

__global__ __launch_bounds__(256) void k_prep(const char* __restrict__ x, const char* __restrict__ wqkv, const char* __restrict__ wg,
                                              const char* __restrict__ wu, char* __restrict__ ws) {
    __shared__ float red[32 * 64];
    const int tid = threadIdx.x, b = blockIdx.x;
    {
        const int row = b * 8 + (tid >> 5), l = tid & 31;
        const char* p = x + (size_t)row * 1280;
        u32x4 v[3];
        v[0] = *(const u32x4*)(p + l * 16); v[1] = *(const u32x4*)(p + 512 + l * 16);
        v[2] = (u32x4){0, 0, 0, 0};
        if (l < 16) v[2] = *(const u32x4*)(p + 1024 + l * 16);
        float s = 0.f;
#pragma unroll
        for (int j = 0; j < 3; ++j)
#pragma unroll
            for (int e = 0; e < 4; ++e) s += bflo(v[j][e]) + bfhi(v[j][e]);
#pragma unroll
        for (int o = 1; o < 32; o <<= 1) s += __shfl_xor(s, o);
        const float mean = s * (1.f / DM);
        float m2 = 0.f;
#pragma unroll
        for (int j = 0; j < 3; ++j)
            if (j < 2 || l < 16) {
#pragma unroll
                for (int e = 0; e < 4; ++e) { float a = bflo(v[j][e]) - mean, c = bfhi(v[j][e]) - mean; m2 += a * a + c * c; }
            }
#pragma unroll
        for (int o = 1; o < 32; o <<= 1) m2 += __shfl_xor(m2, o);
        if (l < 8) *(f32x2*)(ws + WS_ST + ((size_t)row * 8 + l) * 8) = (f32x2){mean, m2 * 0.125f};
    }
    if (b < 156) {
        const int cb = b >> 1, half = b & 1;
        const char* w; int pitch, col0; float* out;
        if (cb < 30) { w = wqkv; pitch = 3840; col0 = cb * 64; out = (float*)(ws + WS_CSQ) + half * 1920 + col0; }
        else if (cb < 54) { w = wg; pitch = 3072; col0 = (cb - 30) * 64; out = (float*)(ws + WS_CSG) + half * FF + col0; }
        else { w = wu; pitch = 3072; col0 = (cb - 54) * 64; out = (float*)(ws + WS_CSU) + half * FF + col0; }
        const int rr = tid >> 3, c = tid & 7;
        const char* p = w + (size_t)(half * 320 + rr) * pitch + col0 * 2 + c * 16;
        u32x4 v[10];
#pragma unroll
        for (int j = 0; j < 10; ++j) v[j] = *(const u32x4*)(p + (size_t)j * 32 * pitch);
        float s[8];
#pragma unroll
        for (int e = 0; e < 8; ++e) s[e] = 0.f;
#pragma unroll
        for (int j = 0; j < 10; ++j)
#pragma unroll
            for (int e = 0; e < 4; ++e) { s[2 * e] += bflo(v[j][e]); s[2 * e + 1] += bfhi(v[j][e]); }
#pragma unroll
        for (int e = 0; e < 8; ++e) red[rr * 64 + c * 8 + e] = s[e];
        __syncthreads();
        if (tid < 64) {
            float t = 0.f;
#pragma unroll
            for (int r = 0; r < 32; ++r) t += red[r * 64 + tid];
            out[tid] = t;
        }
    }
}

template <int MODE> struct LnCfg;
template <> struct LnCfg<0> { static constexpr int NSLOT = 4, SLOT = 32768, NB = 2, PPW = 4, NCS = 128; };
template <> struct LnCfg<1> { static constexpr int NSLOT = 3, SLOT = 41216, NB = 3, PPW = 5, NCS = 192; };
constexpr int GOFF = 16384, UOFF = 16384 + 12288 + 32;

template <int MODE, bool SWAP>
DI void ln_mainloop(LAS char* smem, f32x16 (&acc)[LnCfg<MODE>::NB][2], int wm, int wn, int lane) {
    using C = LnCfg<MODE>;
    constexpr int NB = C::NB;
    const int r = lane & 31, h = lane >> 5, q = (lane & 15) >> 2, pp = lane & 3, blk = (lane >> 4) & 1;
    unsigned xa[4], wa[2];
    { const int row = 64 * wm + r, sw = (row >> 1) & 7;
#pragma unroll
      for (int ks = 0; ks < 4; ++ks) xa[ks] = row * 128 + (((2 * ks + h) ^ sw) << 4); }
    if (MODE == 0) {
#pragma unroll
        for (int cb = 0; cb < 2; ++cb) wa[cb] = 16384 + (8 * h + q) * 256 + (((2 * wn + cb) ^ q) * 64) + blk * 32 + pp * 8;
    } else { wa[0] = (blk ? UOFF : GOFF) + (8 * h + q) * 192 + wn * 96 + pp * 8; wa[1] = 0; }
    bf16x8 xf[2][2], wf[2][NB];
#define LN_READ(BUF, SB, KS) do { \
        _Pragma("unroll") for (int tb = 0; tb < 2; ++tb) xf[BUF][tb] = lds128((SB) + xa[KS] + tb * 4096); \
        _Pragma("unroll") for (int j = 0; j < NB; ++j) { \
            if (MODE == 0) wf[BUF][j] = cat(ldstr((SB) + wa[j] + (KS) * 4096), ldstr((SB) + wa[j] + (KS) * 4096 + 1024)); \
            else wf[BUF][j] = cat(ldstr((SB) + wa[0] + (KS) * 3072 + j * 32), ldstr((SB) + wa[0] + (KS) * 3072 + 768 + j * 32)); } \
    } while (0)
#define LN_MMA(BUF) do { \
        _Pragma("unroll") for (int j = 0; j < NB; ++j) _Pragma("unroll") for (int tb = 0; tb < 2; ++tb) \
            acc[j][tb] = SWAP ? MFMA32(wf[BUF][j], xf[BUF][tb], acc[j][tb]) : MFMA32(xf[BUF][tb], wf[BUF][j], acc[j][tb]); \
    } while (0)
    BAR_LGKM();
    int slot = 0;
    LN_READ(0, smem, 0);
#pragma unroll 1
    for (int s = 0; s < 9; ++s) {
        LAS char* sb = smem + slot * C::SLOT;
        slot = (slot + 1 == C::NSLOT) ? 0 : slot + 1;
        LAS char* nb = smem + slot * C::SLOT;
        LN_READ(1, sb, 1); LN_MMA(0);
        LN_READ(0, sb, 2); LN_MMA(1);
        LN_READ(1, sb, 3); LN_MMA(0);
        BAR_LGKM();
        LN_READ(0, nb, 0); LN_MMA(1);
    }
    {
        LAS char* sb = smem + slot * C::SLOT;
        LN_READ(1, sb, 1); LN_MMA(0);
        LN_READ(0, sb, 2); LN_MMA(1);
        LN_READ(1, sb, 3); LN_MMA(0);
        LN_MMA(1);
    }
#undef LN_READ
#undef LN_MMA
}

template <int MODE>
__global__ __launch_bounds__(768) void k_gemm_ln(const char* __restrict__ X, const char* __restrict__ W0, const char* __restrict__ W1,
                                                 char* __restrict__ ws) {
    using C = LnCfg<MODE>;
    constexpr int TAB = C::NSLOT * C::SLOT;
    __shared__ __attribute__((aligned(1024))) char smem_[TAB + 1024 + C::NCS * 4];
    LAS char* smem = (LAS char*)smem_;
    const int tid = threadIdx.x, lane = tid & 63, w = __builtin_amdgcn_readfirstlane(tid >> 6);
    const int xcd = blockIdx.x & 7, bi = blockIdx.x >> 3, pr = xcd >> 1, e = xcd & 1;
    int tm, tn;
    if (MODE == 0) { const int lin = e * 30 + bi; tm = 4 * pr + (lin & 3); tn = lin >> 2; }
    else { tm = 4 * pr + (bi & 3); tn = 8 * e + (bi >> 2); }

    if (w >= 4) {
        const int j = w - 4;
        unsigned off[C::PPW]; int dst[C::PPW];
#pragma unroll
        for (int p = 0; p < 2; ++p) {
            const int piece = j + 8 * p, row = piece * 8 + (lane >> 3);
            off[p] = (unsigned)((tm * 128 + row) * 1280 + (((lane & 7) ^ ((row >> 1) & 7)) << 4));
            dst[p] = piece * 1024;
        }
        if (MODE == 0) {
#pragma unroll
            for (int p = 0; p < 2; ++p) {
                const int piece = j + 8 * p, k = piece * 4 + (lane >> 4), g = lane & 15;
                off[2 + p] = (unsigned)(k * 3840 + tn * 256 + (((g >> 2) ^ (k & 3)) << 6) + (g & 3) * 16);
                dst[2 + p] = 16384 + piece * 1024;
            }
        } else {
#pragma unroll
            for (int p = 0; p < 3; ++p) {
                const int piece = j + 8 * p, pi = piece >= 12 ? piece - 12 : piece;
                const int G = pi * 64 + lane, k = G / 12, c = G - k * 12;
                off[2 + p] = (unsigned)(k * 3072 + tn * 192 + c * 16);
                dst[2 + p] = (piece >= 12 ? UOFF : GOFF) + pi * 1024;
            }
        }
        constexpr int WPITCH = MODE == 0 ? 3840 : 3072;
        auto issue = [&](int s) {
            int kb = s + 5 * e; if (kb >= 10) kb -= 10;
            LAS char* sb = smem + (s % C::NSLOT) * C::SLOT;
            const char* xb = X + kb * 128;
#pragma unroll
            for (int p = 0; p < 2; ++p) glds16(xb + off[p], sb + dst[p]);
#pragma unroll
            for (int p = 2; p < C::PPW; ++p) {
                const char* wb = (MODE == 1 && (j + 8 * (p - 2)) >= 12) ? W1 : W0;
                glds16(wb + (size_t)kb * 64 * WPITCH + off[p], sb + dst[p]);
            }
        };
#pragma unroll
        for (int s = 0; s < C::NSLOT; ++s) issue(s);
        WAITV((C::NSLOT - 1) * C::PPW);
        BAR_RAW();
#pragma unroll
        for (int s = 0; s < 9; ++s) {
            constexpr int dummy = 0; (void)dummy;
            const int last = (s + C::NSLOT - 1 < 9) ? s + C::NSLOT - 1 : 9;
            switch (last - (s + 1)) {
                case 0: WAITV(0); break;
                case 1: WAITV(C::PPW); break;
                case 2: WAITV(2 * C::PPW); break;
                default: WAITV(3 * C::PPW); break;
            }
            BAR_RAW();
            if (s + C::NSLOT <= 9) issue(s + C::NSLOT);
        }
        return;
    }

    LAS float* tmu = (LAS float*)(smem + TAB);
    LAS float* trs = tmu + 128;
    LAS float* tcs = tmu + 256;
    {
        const int row = tid >> 1, hf = tid & 1;
        const f32x4* sp = (const f32x4*)(ws + WS_ST + ((size_t)(tm * 128 + row) * 8 + hf * 4) * 8);
        const f32x4 a = sp[0], b = sp[1];
        float cs0 = 0.f, cs1 = 0.f;
        if (MODE == 0) { if (tid < 128) { const float* c = (const float*)(ws + WS_CSQ) + tn * 128 + tid; cs0 = c[0]; cs1 = c[1920]; } }
        else if (tid < 192) { const float* c = (const float*)(ws + (tid < 96 ? WS_CSG : WS_CSU)) + tn * 96 + (tid < 96 ? tid : tid - 96); cs0 = c[0]; cs1 = c[FF]; }
        float sm = a[0] + a[2] + b[0] + b[2];
        sm += __shfl_xor(sm, 1);
        const float mu = sm * 0.125f;
        float d0 = a[0] - mu, d1 = a[2] - mu, d2 = b[0] - mu, d3 = b[2] - mu;
        float m2 = a[1] + a[3] + b[1] + b[3] + 80.f * (d0 * d0 + d1 * d1 + d2 * d2 + d3 * d3);
        m2 += __shfl_xor(m2, 1);
        if (hf == 0) { tmu[row] = mu; trs[row] = __builtin_amdgcn_rsqf(m2 * (1.f / DM) + LN_EPS); }
        if (tid < C::NCS) tcs[tid] = cs0 + cs1;
    }
    const int wm = w & 1, wn = w >> 1, r = lane & 31, h = lane >> 5;
    f32x16 acc[C::NB][2];
#pragma unroll
    for (int j = 0; j < C::NB; ++j)
#pragma unroll
        for (int tb = 0; tb < 2; ++tb)
#pragma unroll
            for (int i = 0; i < 16; ++i) acc[j][tb][i] = 0.f;

    if (MODE == 1) {
        ln_mainloop<MODE, true>(smem, acc, wm, wn, lane);
        char* hout = ws + WS_H;
#pragma unroll
        for (int tb = 0; tb < 2; ++tb) {
            const int tl = 64 * wm + 32 * tb + r;
            const float mu = tmu[tl], rs = trs[tl];
            char* rowp = hout + (size_t)(tm * 128 + tl) * (FF * 2) + tn * 192 + wn * 96 + h * 16;
#pragma unroll
            for (int j = 0; j < 3; ++j) {
                unsigned pk[2][2];
#pragma unroll
                for (int qd = 0; qd < 2; ++qd) {
                    const int fl = 16 * (3 * wn + j) + 8 * qd + 4 * h;
                    const f32x4 cg = *(LAS const f32x4*)(tcs + fl), cu = *(LAS const f32x4*)(tcs + 96 + fl);
                    float hv[4];
#pragma unroll
                    for (int i = 0; i < 4; ++i) {
                        const float g = rs * (acc[j][tb][4 * qd + i] - mu * cg[i]);
                        const float u = rs * (acc[j][tb][8 + 4 * qd + i] - mu * cu[i]);
                        hv[i] = fmaxf(g, 0.f) * u;
                    }
                    pk[qd][0] = pack2(hv[0], hv[1]); pk[qd][1] = pack2(hv[2], hv[3]);
                }
                auto s0 = __builtin_amdgcn_permlane32_swap(pk[0][0], pk[1][0], false, false);
                auto s1 = __builtin_amdgcn_permlane32_swap(pk[0][1], pk[1][1], false, false);
                *(u32x4*)(rowp + j * 32) = (u32x4){s0[0], s1[0], s0[1], s1[1]};
            }
        }
    } else if (tn < 10) {
        ln_mainloop<MODE, true>(smem, acc, wm, wn, lane);
        const bool isq = tn < 5;
        const float sc = isq ? QSCALE : 1.f;
#pragma unroll
        for (int tb = 0; tb < 2; ++tb) {
            const int tl = 64 * wm + 32 * tb + r, tok = tm * 128 + tl;
            const float mu = tmu[tl], rs = trs[tl] * sc;
#pragma unroll
            for (int cb = 0; cb < 2; ++cb)
#pragma unroll
                for (int qd = 0; qd < 4; ++qd) {
                    const int nl = 64 * wn + 32 * cb + 8 * qd + 4 * h;
                    const f32x4 cs = *(LAS const f32x4*)(tcs + nl);
                    float v[4];
#pragma unroll
                    for (int i = 0; i < 4; ++i) v[i] = rs * (acc[cb][tb][4 * qd + i] - mu * cs[i]);
                    const u32x2 pk = {pack2(v[0], v[1]), pack2(v[2], v[3])};
                    const int nn = (isq ? tn : tn - 5) * 128 + nl, head = nn / DH, d = nn - head * DH;
                    size_t a;
                    if (isq) a = WS_Q + (size_t)(head * SEQ + tok) * 40 + d * 2;
                    else {
                        const int kb = (tok >> 5) & 7;
                        a = WS_KV + (size_t)(head * 8 + (tok >> 8)) * KVCH +
                            (d < 16 ? kb * 1024 + ((d >> 3) * 32 + r) * 16 + (d & 4) * 2 : KV_K1 + kb * 256 + r * 8);
                    }
                    *(u32x2*)(ws + a) = pk;
                }
        }
    } else {
        ln_mainloop<MODE, false>(smem, acc, wm, wn, lane);
#pragma unroll
        for (int cb = 0; cb < 2; ++cb) {
            const int nl = 64 * wn + 32 * cb + r, nn = (tn - 10) * 128 + nl, head = nn / DH, d = nn - head * DH;
            const float cs = tcs[nl];
#pragma unroll
            for (int tb = 0; tb < 2; ++tb) {
                const int t0 = 64 * wm + 32 * tb, tok0 = tm * 128 + t0, kb = (tok0 >> 5) & 7;
                char* base = ws + WS_KV + (size_t)(head * 8 + (tok0 >> 8)) * KVCH + KV_V + 2 * kb * VSTEP + h * 336 + d * 16;
#pragma unroll
                for (int s = 0; s < 2; ++s) {
                    u32x4 pk;
#pragma unroll
                    for (int qq = 0; qq < 2; ++qq) {
                        const int qd = 2 * s + qq, tl = t0 + 8 * qd + 4 * h;
                        const f32x4 mu = *(LAS const f32x4*)(tmu + tl), rs = *(LAS const f32x4*)(trs + tl);
                        float v[4];
#pragma unroll
                        for (int i = 0; i < 4; ++i) v[i] = rs[i] * (acc[cb][tb][4 * qd + i] - mu[i] * cs);
                        pk[2 * qq] = pack2(v[0], v[1]); pk[2 * qq + 1] = pack2(v[2], v[3]);
                    }
                    *(u32x4*)(base + s * VSTEP) = pk;
                    if (d == DH - 1) *(u32x4*)(base + s * VSTEP + 16) = (u32x4){0x3F803F80u, 0x3F803F80u, 0x3F803F80u, 0x3F803F80u};
                }
            }
        }
    }
}

template <int KT>
__global__ __launch_bounds__(1024) void k_gemm_res(const char* __restrict__ A, const char* __restrict__ W, const char* __restrict__ res,
                                                   char* __restrict__ out, char* __restrict__ ws) {
    constexpr int NS = KT / 128, NSLOT = 4, SLOT = 36864, PPW = 3;
    __shared__ __attribute__((aligned(1024))) char smem_[NSLOT * SLOT];
    LAS char* smem = (LAS char*)smem_;
    const int tid = threadIdx.x, lane = tid & 63, w = __builtin_amdgcn_readfirstlane(tid >> 6);
    const int xcd = blockIdx.x & 7, bi = blockIdx.x >> 3, pr = xcd >> 1, e = xcd & 1;
    const int tm = 8 * pr + (bi & 7), tn = 4 * e + (bi >> 3);

    if (w >= 4) {
        const int j = w - 4;
        unsigned off[PPW]; bool isa[PPW];
#pragma unroll
        for (int p = 0; p < PPW; ++p) {
            const int piece = j + 12 * p;
            isa[p] = piece < 16;
            if (piece < 16) {
                const int row = piece * 4 + (lane >> 4);
                off[p] = (unsigned)((tm * 64 + row) * (KT * 2) + (((lane & 15) ^ (row & 15)) << 4));
            } else {
                const int G = (piece - 16) * 64 + lane, ir = G / 10, c = G - ir * 10;
                const int k = (ir & ~12) | ((ir & 4) << 1) | ((ir & 8) >> 1);
                off[p] = (unsigned)(k * 1280 + tn * 160 + c * 16);
            }
        }
        auto issue = [&](int s) {
            int kb;
            if (NS == 5) kb = e ? 4 - s : s; else { kb = s + (NS / 2) * e; if (kb >= NS) kb -= NS; }
            LAS char* sb = smem + (s % NSLOT) * SLOT;
#pragma unroll
            for (int p = 0; p < PPW; ++p) {
                const char* src = isa[p] ? A + kb * 256 : W + (size_t)kb * 128 * 1280;
                glds16(src + off[p], sb + (j + 12 * p) * 1024);
            }
        };
#pragma unroll
        for (int s = 0; s < NSLOT; ++s) issue(s);
        WAITV((NSLOT - 1) * PPW);
        BAR_RAW();
#pragma unroll
        for (int s = 0; s < NS - 1; ++s) {
            const int last = (s + NSLOT - 1 < NS - 1) ? s + NSLOT - 1 : NS - 1;
            switch (last - (s + 1)) {
                case 0: WAITV(0); break;
                case 1: WAITV(PPW); break;
                default: WAITV(2 * PPW); break;
            }
            BAR_RAW();
            if (s + NSLOT <= NS - 1) issue(s + NSLOT);
        }
        return;
    }

    const int r = lane & 15, g = lane >> 4, q = (lane & 15) >> 2, pp = lane & 3;
    const int tok = tm * 64 + 16 * w + r;
    u32x2 rv[5];
#pragma unroll
    for (int j = 0; j < 5; ++j) rv[j] = *(const u32x2*)(res + (size_t)tok * 1280 + tn * 160 + j * 32 + g * 8);
    unsigned xa[4];
    { const int row = 16 * w + r;
#pragma unroll
      for (int ks = 0; ks < 4; ++ks) xa[ks] = row * 256 + (((4 * ks + g) ^ r) << 4); }
    const unsigned wa = 16384 + (16 * (g >> 1) + 4 * (g & 1) + q) * 160 + pp * 8;
    f32x4 acc[5];
#pragma unroll
    for (int j = 0; j < 5; ++j) acc[j] = (f32x4){0.f, 0.f, 0.f, 0.f};
    bf16x8 xf[2], wf[2][5];
#define RS_READ(BUF, SB, KS) do { xf[BUF] = lds128((SB) + xa[KS]); \
        _Pragma("unroll") for (int j = 0; j < 5; ++j) wf[BUF][j] = cat(ldstr((SB) + wa + (KS) * 5120 + j * 32), ldstr((SB) + wa + (KS) * 5120 + 1280 + j * 32)); } while (0)
#define RS_MMA(BUF) do { _Pragma("unroll") for (int j = 0; j < 5; ++j) acc[j] = MFMA16(wf[BUF][j], xf[BUF], acc[j]); } while (0)
    BAR_LGKM();
    int slot = 0;
    RS_READ(0, smem, 0);
#pragma unroll 1
    for (int s = 0; s < NS - 1; ++s) {
        LAS char* sb = smem + slot * SLOT;
        slot = (slot + 1) & (NSLOT - 1);
        LAS char* nb = smem + slot * SLOT;
        RS_READ(1, sb, 1); RS_MMA(0);
        RS_READ(0, sb, 2); RS_MMA(1);
        RS_READ(1, sb, 3); RS_MMA(0);
        BAR_LGKM();
        RS_READ(0, nb, 0); RS_MMA(1);
    }
    {
        LAS char* sb = smem + slot * SLOT;
        RS_READ(1, sb, 1); RS_MMA(0);
        RS_READ(0, sb, 2); RS_MMA(1);
        RS_READ(1, sb, 3); RS_MMA(0);
        RS_MMA(1);
    }
#undef RS_READ
#undef RS_MMA
    float v[20]; float sum = 0.f;
#pragma unroll
    for (int j = 0; j < 5; ++j) {
        const u32x2 pk = {pack2(acc[j][0] + bflo(rv[j][0]), acc[j][1] + bfhi(rv[j][0])), pack2(acc[j][2] + bflo(rv[j][1]), acc[j][3] + bfhi(rv[j][1]))};
        *(u32x2*)(out + (size_t)tok * 1280 + tn * 160 + j * 32 + g * 8) = pk;
        v[4 * j] = bflo(pk[0]); v[4 * j + 1] = bfhi(pk[0]); v[4 * j + 2] = bflo(pk[1]); v[4 * j + 3] = bfhi(pk[1]);
        sum += (v[4 * j] + v[4 * j + 1]) + (v[4 * j + 2] + v[4 * j + 3]);
    }
    sum += __shfl_xor(sum, 16); sum += __shfl_xor(sum, 32);
    const float mean = sum * (1.f / 80.f);
    float m2 = 0.f;
#pragma unroll
    for (int i = 0; i < 20; ++i) { const float d = v[i] - mean; m2 += d * d; }
    m2 += __shfl_xor(m2, 16); m2 += __shfl_xor(m2, 32);
    if (g == 0) *(f32x2*)(ws + WS_ST + ((size_t)tok * 8 + tn) * 8) = (f32x2){mean, m2};
}

DI void attn_store(char* O, int tok, int head, int h, const f32x16& o, float inv) {
    char* p = O + (size_t)tok * 1280 + head * 40 + h * 8;
    *(u32x2*)(p) = (u32x2){pack2(o[0] * inv, o[1] * inv), pack2(o[2] * inv, o[3] * inv)};
    *(u32x2*)(p + 16) = (u32x2){pack2(o[4] * inv, o[5] * inv), pack2(o[6] * inv, o[7] * inv)};
    if (h == 0) *(u32x2*)(p + 32) = (u32x2){pack2(o[8] * inv, o[9] * inv), pack2(o[10] * inv, o[11] * inv)};
}
DI float swap_max(float m) { auto rr = __builtin_amdgcn_permlane32_swap(__float_as_uint(m), __float_as_uint(m), false, false); return fmaxf(__uint_as_float(rr[0]), __uint_as_float(rr[1])); }
DI float max16(const f32x16& s) {
    float a = fmaxf(fmaxf(s[0], s[1]), s[2]), b = fmaxf(fmaxf(s[3], s[4]), s[5]);
    a = fmaxf(fmaxf(a, s[6]), s[7]); b = fmaxf(fmaxf(b, s[8]), s[9]);
    a = fmaxf(fmaxf(a, s[10]), s[11]); b = fmaxf(fmaxf(b, s[12]), s[13]);
    a = fmaxf(fmaxf(a, s[14]), s[15]);
    return swap_max(fmaxf(a, b));
}
DI bf16x8 packp(const f32x16& p, int s) {
    u32x4 u = {pack2(p[8 * s], p[8 * s + 1]), pack2(p[8 * s + 2], p[8 * s + 3]), pack2(p[8 * s + 4], p[8 * s + 5]), pack2(p[8 * s + 6], p[8 * s + 7])};
    return __builtin_bit_cast(bf16x8, u);
}

__global__ __launch_bounds__(1024) void k_attn(char* __restrict__ ws) {
    constexpr int NSL = 7, QOFF = NSL * KVCH;
    __shared__ __attribute__((aligned(1024))) char smem_[QOFF + 10240];
    LAS char* smem = (LAS char*)smem_;
    const int tid = threadIdx.x, lane = tid & 63, w = __builtin_amdgcn_readfirstlane(tid >> 6);
    const int xcd = blockIdx.x & 7, bi = blockIdx.x >> 3, pr = xcd >> 1, e = xcd & 1;
    const int head = 16 * e + (bi >> 1), qt = 2 * pr + (bi & 1);
    const char* kv = ws + WS_KV + (size_t)head * 8 * KVCH;
    const int r = lane & 31, h = lane >> 5, qb = w & 7, kh = w >> 3;

    if (w < 10) glds16(ws + WS_Q + (size_t)(head * SEQ + qt * 256) * 40 + w * 1024 + lane * 16, smem + QOFF + w * 1024);
#pragma unroll
    for (int i = 0; i < 9; ++i) glds16(kv + (w + 16 * i) * 1024 + lane * 16, smem + (w + 16 * i) * 1024);
    if (w < 3) glds16(kv + (w + 144) * 1024 + lane * 16, smem + (w + 144) * 1024);
    WAITV(7);
    BAR_RAW();

    bf16x8 q0; bf16x4 q1;
    { LAS const char* qp = smem + QOFF + (qb * 32 + r) * 40;
      q0 = cat(lds64(qp + h * 16), lds64(qp + h * 16 + 8));
      q1 = lds64(qp + 32);
      if (h) q1 = (bf16x4){0, 0, 0, 0}; }
    const unsigned k0a = lane * 16, k1a = KV_K1 + r * 8;
    const unsigned va = KV_V + h * 336 + (r <= 20 ? r : (r < 28 ? 20 : r - 12)) * 16;
    f32x16 zero, negm, o;
#pragma unroll
    for (int i = 0; i < 16; ++i) { zero[i] = 0.f; o[i] = 0.f; }
    {
        f32x16 s = MFMA32(lds128(smem + k0a), q0, zero);
        s = MFMA32K8(lds64(smem + k1a), q1, s);
        const float m = max16(s);
#pragma unroll
        for (int i = 0; i < 16; ++i) negm[i] = -m;
    }
    LAS const char* pk0 = smem + k0a + kh * 4096;
    LAS const char* pk1 = smem + k1a + kh * 1024;
    LAS const char* pv = smem + va + kh * 8 * VSTEP;
#define ATT_STEP(SN, SC, KOFF, KB_N, KB_C) do { \
        const bf16x8 ka = lds128(pk0 + (KOFF) + (KB_N) * 1024); const bf16x4 kb_ = lds64(pk1 + (KOFF) + (KB_N) * 256); \
        const bf16x8 v0 = lds128(pv + (2 * (KB_C)) * VSTEP), v1 = lds128(pv + (2 * (KB_C) + 1) * VSTEP); \
        _Pragma("unroll") for (int i = 0; i < 8; ++i) SC[i] = __builtin_amdgcn_exp2f(SC[i]); \
        const bf16x8 p0 = packp(SC, 0); \
        SN = MFMA32(ka, q0, negm); \
        SN = MFMA32K8(kb_, q1, SN); \
        o = MFMA32(v0, p0, o); \
        _Pragma("unroll") for (int i = 8; i < 16; ++i) SC[i] = __builtin_amdgcn_exp2f(SC[i]); \
        o = MFMA32(v1, packp(SC, 1), o); \
        __builtin_amdgcn_sched_barrier(0); \
    } while (0)
#define ATT_RUN(NCH) do { \
        f32x16 sA = MFMA32(lds128(pk0), q0, negm), sB; \
        sA = MFMA32K8(lds64(pk1), q1, sA); \
        _Pragma("unroll 1") for (int c = 0; c < (NCH); ++c) { \
            ATT_STEP(sB, sA, 0, 1, 0); ATT_STEP(sA, sB, 0, 2, 1); ATT_STEP(sB, sA, 0, 3, 2); ATT_STEP(sA, sB, KVCH, 0, 3); \
            pk0 += KVCH; pk1 += KVCH; pv += KVCH; \
        } } while (0)
    ATT_RUN(1);
    asm volatile("s_waitcnt vmcnt(0) lgkmcnt(0)\n\ts_barrier" ::: "memory");
    glds16(kv + 7 * KVCH + w * 1024 + lane * 16, smem + w * 1024);
    if (w < 5) glds16(kv + 7 * KVCH + (w + 16) * 1024 + lane * 16, smem + (w + 16) * 1024);
    ATT_RUN(6);
    asm volatile("s_waitcnt vmcnt(0) lgkmcnt(0)\n\ts_barrier" ::: "memory");
    pk0 -= 7 * KVCH; pk1 -= 7 * KVCH; pv -= 7 * KVCH;
    ATT_RUN(1);
#undef ATT_RUN
#undef ATT_STEP
    LAS f32x4* cb = (LAS f32x4*)(smem + KVCH) + qb * 192 + lane;
    if (kh == 1) {
#pragma unroll
        for (int i = 0; i < 3; ++i) cb[i * 64] = (f32x4){o[4 * i], o[4 * i + 1], o[4 * i + 2], o[4 * i + 3]};
    }
    BAR_LGKM();
    if (kh == 1) return;
#pragma unroll
    for (int i = 0; i < 3; ++i) { const f32x4 t = cb[i * 64]; o[4 * i] += t[0]; o[4 * i + 1] += t[1]; o[4 * i + 2] += t[2]; o[4 * i + 3] += t[3]; }
    float l;
    { auto rr = __builtin_amdgcn_permlane32_swap(__float_as_uint(o[8]), __float_as_uint(o[8]), false, false); l = __uint_as_float(rr[1]); }
    float chk = 0.f;
#pragma unroll
    for (int i = 0; i < 12; ++i) chk += o[i] * 0.f;
    const bool ok = (chk == 0.f) && (l <= 1.8446744e19f);
    const int tok = qt * 256 + qb * 32 + r;
    if (__builtin_expect(!__all(ok), 0)) {
        float m = -INFINITY;
#pragma unroll
        for (int i = 0; i < 16; ++i) o[i] = 0.f;
#pragma unroll 1
        for (int kb = 0; kb < 64; ++kb) {
            const char* cp = kv + (size_t)(kb >> 3) * KVCH; const int u = kb & 7;
            f32x16 s = MFMA32(*(const bf16x8*)(cp + k0a + u * 1024), q0, zero);
            s = MFMA32K8(*(const bf16x4*)(cp + k1a + u * 256), q1, s);
            const float mn = fmaxf(m, max16(s));
            const float al = __builtin_amdgcn_exp2f(m - mn);
            m = mn;
#pragma unroll
            for (int i = 0; i < 16; ++i) { o[i] *= al; s[i] = __builtin_amdgcn_exp2f(s[i] - mn); }
            o = MFMA32(*(const bf16x8*)(cp + va + (2 * u) * VSTEP), packp(s, 0), o);
            o = MFMA32(*(const bf16x8*)(cp + va + (2 * u + 1) * VSTEP), packp(s, 1), o);
        }
        auto rr = __builtin_amdgcn_permlane32_swap(__float_as_uint(o[8]), __float_as_uint(o[8]), false, false); l = __uint_as_float(rr[1]);
    }
    attn_store(ws + WS_O, tok, head, h, o, 1.f / l);
}

extern "C" void kernel_launch(void* const* d_in, const int* in_sizes, int n_in, void* d_out, int out_size, void* d_ws, size_t ws_size,
                              hipStream_t stream) {
    const char* x0 = (const char*)d_in[0];
    const char* wqkv = (const char*)d_in[1];
    const char* wo = (const char*)d_in[2];
    const char* wg = (const char*)d_in[4];
    const char* wu = (const char*)d_in[5];
    const char* wd = (const char*)d_in[6];
    char* ws = (char*)d_ws;
    char* out = (char*)d_out;
    k_prep<<<256, 256, 0, stream>>>(x0, wqkv, wg, wu, ws);
    for (int layer = 0; layer < 2; ++layer) {
        const char* xin = layer == 0 ? x0 : ws + WS_X2;
        char* xa = ws + (layer == 0 ? WS_X1 : WS_X3);
        char* xb = layer == 0 ? ws + WS_X2 : out;
        k_gemm_ln<0><<<240, 768, 0, stream>>>(xin, wqkv, wqkv, ws);
        k_attn<<<256, 1024, 0, stream>>>(ws);
        k_gemm_res<DM><<<256, 1024, 0, stream>>>(ws + WS_O, wo, xin, xa, ws);
        k_gemm_ln<1><<<256, 768, 0, stream>>>(xa, wg, wu, ws);
        k_gemm_res<FF><<<256, 1024, 0, stream>>>(ws + WS_H, wd, xa, xb, ws);
    }
}
```

```cpp
#include <hip/hip_runtime.h>
#include <hip/hip_bf16.h>

typedef short bf16x8 __attribute__((ext_vector_type(8)));
typedef short bf16x4 __attribute__((ext_vector_type(4)));
typedef float f32x16 __attribute__((ext_vector_type(16)));
typedef float f32x4 __attribute__((ext_vector_type(4)));
typedef float f32x2 __attribute__((ext_vector_type(2)));
typedef unsigned u32x4 __attribute__((ext_vector_type(4)));
typedef unsigned u32x2 __attribute__((ext_vector_type(2)));
typedef __bf16 bf2_t __attribute__((ext_vector_type(2)));
#define LAS __attribute__((address_space(3)))
#define DI __device__ __forceinline__
#define MFMA32(a, b, c) __builtin_amdgcn_mfma_f32_32x32x16_bf16((a), (b), (c), 0, 0, 0)
#define MFMA32K8(a, b, c) __builtin_amdgcn_mfma_f32_32x32x8bf16_1k((a), (b), (c), 0, 0, 0)
#define MFMA16(a, b, c) __builtin_amdgcn_mfma_f32_16x16x32_bf16((a), (b), (c), 0, 0, 0)
#define WAITV(n) asm volatile("s_waitcnt vmcnt(%0)" ::"i"(n) : "memory")
#define BAR_LGKM() asm volatile("s_waitcnt lgkmcnt(0)\n\ts_barrier" ::: "memory")
#define BAR_RAW() asm volatile("s_barrier" ::: "memory")

constexpr int SEQ = 2048, DM = 640, NH = 32, DH = 20, FF = 1536;
constexpr int KVCH = 21504;
constexpr int KV_K1 = 8192, KV_V = 10240, VSTEP = 704, VHALF = 352;
constexpr float QSCALE = 0.22360679774997896f * 1.4426950408889634f;
constexpr float LN_EPS = 1e-5f;

constexpr size_t WS_X1 = 0, WS_X2 = WS_X1 + (size_t)SEQ * DM * 2, WS_X3 = WS_X2 + (size_t)SEQ * DM * 2;
constexpr size_t WS_Q = WS_X3 + (size_t)SEQ * DM * 2, WS_O = WS_Q + (size_t)SEQ * DM * 2;
constexpr size_t WS_KV = WS_O + (size_t)SEQ * DM * 2, WS_H = WS_KV + (size_t)NH * 8 * KVCH;
constexpr size_t WS_ST = WS_H + (size_t)SEQ * FF * 2;
constexpr size_t WS_CSQ = WS_ST + (size_t)SEQ * 8 * 8;
constexpr size_t WS_CSG = WS_CSQ + 2 * 1920 * 4, WS_CSU = WS_CSG + 2 * FF * 4;
constexpr size_t WS_WO = (WS_CSU + 2 * FF * 4 + 4095) & ~(size_t)4095;
constexpr size_t WS_WD = WS_WO + (size_t)DM * DM * 2;
constexpr size_t WS_WGU = WS_WD + (size_t)FF * DM * 2;

#define DBG_ARG
#define DBG_PASS(k)
#define STAMP(i)
#define STAMPW(i, wv)
#define TOUCHA 0
#define TOUCHW 0
DI unsigned pack2(float a, float b) { f32x2 v = {a, b}; return __builtin_bit_cast(unsigned, __builtin_convertvector(v, bf2_t)); }
DI float bflo(unsigned u) { return __uint_as_float(u << 16); }
DI float bfhi(unsigned u) { return __uint_as_float(u & 0xffff0000u); }
#define glds16g glds16
DI void glds16(const char* g, LAS char* l) { __builtin_amdgcn_global_load_lds((const __attribute__((address_space(1))) void*)g, (LAS void*)l, 16, 0, 0); }
DI void glds16s(const char* sb_, unsigned voff, unsigned ldsaddr) {
    const unsigned long long sbase = ((unsigned long long)(unsigned)__builtin_amdgcn_readfirstlane((int)((unsigned long long)sb_ >> 32)) << 32) |
                                     (unsigned)__builtin_amdgcn_readfirstlane((int)(unsigned long long)sb_);
    ldsaddr = __builtin_amdgcn_readfirstlane(ldsaddr);
    asm volatile("s_mov_b32 m0, %2\n\ts_nop 3\n\tglobal_load_lds_dwordx4 %1, %0" :: "s"(sbase), "v"(voff), "s"(ldsaddr) : "memory");
}
DI void touch(const char* g, LAS char* l) { __builtin_amdgcn_global_load_lds((const __attribute__((address_space(1))) void*)g, (LAS void*)l, 4, 0, 0); }
DI unsigned wqkv_line(int bi, int e, int t) { const int row = t / 18, c = t - row * 18, sec = c / 6; return (unsigned)((bi * 20 + row) * 3840 + sec * 1280 + e * 512 + (c - sec * 6) * 128); }
DI bf16x8 lds128(LAS const char* p) { return *(LAS const bf16x8*)p; }
DI bf16x4 lds64(LAS const char* p) { return *(LAS const bf16x4*)p; }
DI bf16x4 ldstr(LAS const char* p) { return __builtin_bit_cast(bf16x4, __builtin_amdgcn_ds_read_tr16_b64_v4i16((LAS bf16x4*)p)); }
DI bf16x8 cat(bf16x4 a, bf16x4 b) { return __builtin_shufflevector(a, b, 0, 1, 2, 3, 4, 5, 6, 7); }

__global__ __launch_bounds__(896) void k_prep(const char* __restrict__ x, const char* __restrict__ wqkv, const char* __restrict__ wo,
                                              const char* __restrict__ wg, const char* __restrict__ wu, const char* __restrict__ wd,
                                              char* __restrict__ ws DBG_ARG) {
    __shared__ __attribute__((aligned(1024))) char smem_[5 * 6144 + 32 * 64 * 4];
    LAS char* smem = (LAS char*)smem_;
    LAS float* red = (LAS float*)(smem + 5 * 6144);
    STAMP(0);
    const int tid = threadIdx.x, lane = tid & 63, b = blockIdx.x, w = __builtin_amdgcn_readfirstlane(tid >> 6);
    const int xcd = b & 7, bi = b >> 3, pr = xcd >> 1, e = xcd & 1;
    if (w < 5) {
        const int task = b + 256 * w;
        LAS char* sl = smem + w * 6144;
        const int q = (lane & 15) >> 2, pp = lane & 3;
        if (task < 544) {
            const bool iso = task < 160; const int t2 = iso ? task : task - 160, nk = iso ? 20 : 48, tn = t2 / nk, ks = t2 - tn * nk;
            const char* src = (iso ? wo : wd) + (size_t)ks * 32 * 1280 + tn * 160;
#pragma unroll
            for (int p = 0; p < 5; ++p) { const int G = p * 64 + lane, k = G / 10, c = G - k * 10; glds16(src + k * 1280 + c * 16, sl + p * 1024); }
            WAITV(0);
            const int g = lane >> 4;
            LAS const char* rp = sl + (8 * g + q) * 160 + pp * 8;
            char* dst = ws + (iso ? WS_WO : WS_WD) + (size_t)t2 * 5120 + lane * 16;
#pragma unroll
            for (int j = 0; j < 5; ++j) *(bf16x8*)(dst + j * 1024) = cat(ldstr(rp + j * 32), ldstr(rp + 640 + j * 32));
        } else if (task < 1184) {
            const int t2 = task - 544, tf = t2 / 40, ks = t2 - tf * 40;
            const size_t so = (size_t)ks * 16 * 3072 + tf * 192;
#pragma unroll
            for (int p = 0; p < 3; ++p) { const int G = p * 64 + lane, k = G / 12, c = G - k * 12;
                glds16(wg + so + k * 3072 + c * 16, sl + p * 1024); glds16(wu + so + k * 3072 + c * 16, sl + 3072 + p * 1024); }
            WAITV(0);
            const int h = lane >> 5, blk = (lane >> 4) & 1;
            LAS const char* rp = sl + blk * 3072 + (8 * h + q) * 192 + pp * 8;
            char* dst = ws + WS_WGU + (size_t)t2 * 6144 + lane * 16;
#pragma unroll
            for (int j = 0; j < 6; ++j) *(bf16x8*)(dst + j * 1024) = cat(ldstr(rp + j * 32), ldstr(rp + 768 + j * 32));
        }
    } else if (w < 9) {
        if (b < 156) {
            const int t = tid - 320, cb = b >> 1, half = b & 1;
            const char* wp; int pitch, col0;
            if (cb < 30) { wp = wqkv; pitch = 3840; col0 = cb * 64; }
            else if (cb < 54) { wp = wg; pitch = 3072; col0 = (cb - 30) * 64; }
            else { wp = wu; pitch = 3072; col0 = (cb - 54) * 64; }
            const int rr = t >> 3, c = t & 7;
            const char* p = wp + (size_t)(half * 320 + rr) * pitch + col0 * 2 + c * 16;
            u32x4 v[10];
#pragma unroll
            for (int j = 0; j < 10; ++j) v[j] = *(const u32x4*)(p + (size_t)j * 32 * pitch);
            float s[8];
#pragma unroll
            for (int i = 0; i < 8; ++i) s[i] = 0.f;
#pragma unroll
            for (int j = 0; j < 10; ++j)
#pragma unroll
                for (int i = 0; i < 4; ++i) { s[2 * i] += bflo(v[j][i]); s[2 * i + 1] += bfhi(v[j][i]); }
#pragma unroll
            for (int i = 0; i < 8; ++i) red[rr * 64 + c * 8 + i] = s[i];
        }
    } else if (w < 13) {
        const int t = tid - 576, row = 512 * pr + 16 * bi + 8 * e + (t >> 5), l = t & 31;
        const char* p = x + (size_t)row * 1280;
        u32x4 v[3];
        v[0] = *(const u32x4*)(p + l * 16); v[1] = *(const u32x4*)(p + 512 + l * 16);
        v[2] = (u32x4){0, 0, 0, 0};
        if (l < 16) v[2] = *(const u32x4*)(p + 1024 + l * 16);
        float s = 0.f;
#pragma unroll
        for (int j = 0; j < 3; ++j)
#pragma unroll
            for (int i = 0; i < 4; ++i) s += bflo(v[j][i]) + bfhi(v[j][i]);
#pragma unroll
        for (int o = 1; o < 32; o <<= 1) s += __shfl_xor(s, o);
        const float mean = s * (1.f / DM);
        float m2 = 0.f;
#pragma unroll
        for (int j = 0; j < 3; ++j)
            if (j < 2 || l < 16) {
#pragma unroll
                for (int i = 0; i < 4; ++i) { float a = bflo(v[j][i]) - mean, c = bfhi(v[j][i]) - mean; m2 += a * a + c * c; }
            }
#pragma unroll
        for (int o = 1; o < 32; o <<= 1) m2 += __shfl_xor(m2, o);
        if (l < 8) *(f32x2*)(ws + WS_ST + ((size_t)row * 8 + l) * 8) = (f32x2){mean, m2 * 0.125f};
    } else {
        unsigned tv = 0;
#pragma unroll
        for (int i = 0; i < 6; ++i) { const int t = i * 64 + lane; if (t < 360) tv ^= *(const unsigned*)(wqkv + wqkv_line(bi, e, t)); }
        const char* xp = x + (size_t)(512 * pr + 16 * bi + 8 * (1 - e)) * 1280;
        tv ^= *(const unsigned*)(xp + lane * 128);
        tv ^= *(const unsigned*)(xp + 8192 + (lane & 15) * 128);
        asm volatile("" ::"v"(tv));
    }
    BAR_LGKM();
    if (b < 156 && w == 5) {
        const int cb = b >> 1, half = b & 1;
        float* out = cb < 30 ? (float*)(ws + WS_CSQ) + half * 1920 + cb * 64
                             : (cb < 54 ? (float*)(ws + WS_CSG) + half * FF + (cb - 30) * 64 : (float*)(ws + WS_CSU) + half * FF + (cb - 54) * 64);
        float t = 0.f;
#pragma unroll
        for (int r = 0; r < 32; ++r) t += red[r * 64 + lane];
        out[lane] = t;
    }
    STAMP(3);
}

template <int MODE> struct LnCfg;
template <> struct LnCfg<0> { static constexpr int SLOT = 32768, NB = 2, PPW = 4, NCS = 128; };
template <> struct LnCfg<1> { static constexpr int SLOT = 40960, NB = 3, PPW = 5, NCS = 192; };

template <int MODE, bool SWAP>
DI void ln_mainloop(LAS char* smem, f32x16 (&acc)[LnCfg<MODE>::NB][2], int wm, int wn, int lane, const char* ta, const char* tw, int tws) {
    using C = LnCfg<MODE>;
    constexpr int NB = C::NB;
    const int r = lane & 31, h = lane >> 5, q = (lane & 15) >> 2, pp = lane & 3, blk = (lane >> 4) & 1;
    unsigned xa[4], wa[2];
    { const int row = 64 * wm + r, sw = (row >> 1) & 7;
#pragma unroll
      for (int ks = 0; ks < 4; ++ks) xa[ks] = row * 128 + (((2 * ks + h) ^ sw) << 4); }
    if (MODE == 0) {
#pragma unroll
        for (int cb = 0; cb < 2; ++cb) wa[cb] = 16384 + (8 * h + q) * 256 + (((2 * wn + cb) ^ q) * 64) + blk * 32 + pp * 8;
    } else { wa[0] = 16384 + wn * 3072 + lane * 16; wa[1] = 0; }
    bf16x8 xf[2][2], wf[2][NB];
#define LN_READ(BUF, SB, KS) do { \
        _Pragma("unroll") for (int tb = 0; tb < 2; ++tb) xf[BUF][tb] = lds128((SB) + xa[KS] + tb * 4096); \
        _Pragma("unroll") for (int j = 0; j < NB; ++j) { \
            if (MODE == 0) wf[BUF][j] = cat(ldstr((SB) + wa[j] + (KS) * 4096), ldstr((SB) + wa[j] + (KS) * 4096 + 1024)); \
            else wf[BUF][j] = lds128((SB) + wa[0] + (KS) * 6144 + j * 1024); } \
    } while (0)
#define LN_MMA(BUF) do { \
        _Pragma("unroll") for (int j = 0; j < NB; ++j) _Pragma("unroll") for (int tb = 0; tb < 2; ++tb) \
            acc[j][tb] = SWAP ? MFMA32(wf[BUF][j], xf[BUF][tb], acc[j][tb]) : MFMA32(xf[BUF][tb], wf[BUF][j], acc[j][tb]); \
    } while (0)
#define LN_STEP(RB, SB, KS, MB) do { LN_READ(RB, SB, KS); LN_MMA(MB); \
        _Pragma("unroll") for (int i_ = 0; i_ < 3; ++i_) { __builtin_amdgcn_sched_group_barrier(0x8, 1, 0); __builtin_amdgcn_sched_group_barrier(0x100, 2, 0); } \
        __builtin_amdgcn_sched_group_barrier(0x8, 2 * NB - 3, 0); \
        __builtin_amdgcn_sched_barrier(0); } while (0)
    BAR_RAW();
    LN_READ(0, smem, 0);
    unsigned tv = 0;
    if (TOUCHA) tv = *(const unsigned*)ta;
#pragma unroll 1
    for (int s = 0; s < 9; ++s) {
        LAS char* sb = smem + (s & 3) * C::SLOT;
        LAS char* nb = smem + ((s + 1) & 3) * C::SLOT;
        __builtin_amdgcn_sched_barrier(0);
        LN_STEP(1, sb, 1, 0);
        LN_STEP(0, sb, 2, 1);
        LN_STEP(1, sb, 3, 0);
        BAR_RAW();
        if (TOUCHW && MODE == 1 && s == tws) { asm volatile("" ::"v"(tv)); tv = *(const unsigned*)tw; }
        __builtin_amdgcn_sched_barrier(0);
        LN_STEP(0, nb, 0, 1);
    }
    {
        LAS char* sb = smem + C::SLOT;
        __builtin_amdgcn_sched_barrier(0);
        LN_STEP(1, sb, 1, 0);
        LN_STEP(0, sb, 2, 1);
        LN_STEP(1, sb, 3, 0);
        BAR_RAW();
        LN_MMA(1);
    }
    asm volatile("" ::"v"(tv));
#undef LN_STEP
#undef LN_READ
#undef LN_MMA
}

template <int MODE>
__global__ __launch_bounds__(768) void k_gemm_ln(const char* __restrict__ X, const char* __restrict__ W0, char* __restrict__ ws DBG_ARG) {
    STAMP(0);
    using C = LnCfg<MODE>;
    __shared__ __attribute__((aligned(1024))) char smem_[4 * C::SLOT];
    LAS char* smem = (LAS char*)smem_;
    const int tid = threadIdx.x, lane = tid & 63, w = __builtin_amdgcn_readfirstlane(tid >> 6);
    const int xcd = blockIdx.x & 7, bi = blockIdx.x >> 3, pr = xcd >> 1, e = xcd & 1;
    int tm, tn;
    if (MODE == 0) {
        const int sec = bi / 10, idx = bi - sec * 10;
        tm = 4 * pr + (idx < 8 ? (idx & 3) : 2 * e + idx - 8); tn = 5 * sec + (idx < 8 ? 3 * e + (idx >> 2) : 2);
    }
    else { tm = 4 * pr + (bi & 3); tn = 8 * e + (bi >> 2); }
    LAS float* tmu = (LAS float*)(smem + 3 * C::SLOT);
    LAS float* trs = tmu + 128;
    LAS float* tcs = tmu + 256;

    if (w < 8) {
        const int j = w;
        unsigned off[C::PPW];
#pragma unroll
        for (int p = 0; p < 2; ++p) {
            const int row = (j + 8 * p) * 8 + (lane >> 3);
            off[p] = (unsigned)((tm * 128 + row) * 1280 + (((lane & 7) ^ ((row >> 1) & 7)) << 4));
        }
#pragma unroll
        for (int p = 2; p < C::PPW; ++p) {
            const int piece = j + 8 * (p - 2);
            if (MODE == 0) { const int k = piece * 4 + (lane >> 4), g = lane & 15;
                off[p] = (unsigned)(k * 3840 + tn * 256 + (((g >> 2) ^ (k & 3)) << 6) + (g & 3) * 16); }
            else off[p] = (unsigned)(tn * 245760 + piece * 1024 + lane * 16);
        }
        constexpr int WSTAGE = MODE == 0 ? 64 * 3840 : 24576;
        int kb = 5 * e;
        auto issue = [&](int s) {
            const unsigned sb = (unsigned)(size_t)smem + (s & 3) * C::SLOT + j * 1024;
#pragma unroll
            for (int p = 0; p < C::PPW; ++p) glds16s(p < 2 ? X + kb * 128 : W0 + kb * WSTAGE, off[p], sb + p * 8192);
            kb = (kb == 9) ? 0 : kb + 1;
        };
        issue(0); issue(1);
        asm volatile("" ::: "memory");
        const f32x4 st = *(const f32x4*)(ws + WS_ST + (size_t)tm * 8192 + tid * 16);
        const int ct = tid < C::NCS ? tid : 0;
        const float* cp = MODE == 0 ? (const float*)(ws + WS_CSQ) + tn * 128 + ct
                                    : (const float*)(ws + (ct < 96 ? WS_CSG : WS_CSU)) + tn * 96 + (ct < 96 ? ct : ct - 96);
        const float cs0 = cp[0], cs1 = cp[MODE == 0 ? 1920 : FF];
        WAITV(C::PPW + 3);
        BAR_RAW();
#pragma unroll 1
        for (int s = 0; s < 8; ++s) {
            issue(s + 2);
            WAITV(C::PPW);
            BAR_RAW();
        }
        WAITV(0);
        BAR_RAW();
        {
            float sm = st[0] + st[2];
            sm += __shfl_xor(sm, 1); sm += __shfl_xor(sm, 2);
            const float mu = sm * 0.125f, d0 = st[0] - mu, d1 = st[2] - mu;
            float m2 = st[1] + st[3] + 80.f * (d0 * d0 + d1 * d1);
            m2 += __shfl_xor(m2, 1); m2 += __shfl_xor(m2, 2);
            if ((tid & 3) == 0) { tmu[tid >> 2] = mu; trs[tid >> 2] = __builtin_amdgcn_rsqf(m2 * (1.f / DM) + LN_EPS); }
            if (tid < C::NCS) tcs[tid] = cs0 + cs1;
        }
        BAR_LGKM();
        return;
    }

    const int wm = w & 1, wn = (w >> 1) & 1, r = lane & 31, h = lane >> 5;
    const char *ta, *tw;
    { const int t0 = tid - 512, t = (t0 & 63) < 23 ? (t0 >> 6) * 23 + (t0 & 63) : 0, rr = min(bi * 18 + t / 5, 511);
      ta = X + (size_t)(512 * pr + rr) * 1280 + (1 - e) * 640 + (t % 5) * 128;
      tw = ws + WS_WD + (size_t)e * 983040 + bi * 30720 + ((t0 >> 6) * 60 + min(t0 & 63, 59)) * 128; }
    const int tws = 2 + (w - 8);
    f32x16 acc[C::NB][2];
#pragma unroll
    for (int j = 0; j < C::NB; ++j)
#pragma unroll
        for (int tb = 0; tb < 2; ++tb)
#pragma unroll
            for (int i = 0; i < 16; ++i) acc[j][tb][i] = 0.f;

    if (MODE == 1) {
        STAMPW(1, 8); ln_mainloop<MODE, true>(smem, acc, wm, wn, lane, ta, tw, tws); STAMPW(2, 8);
        char* hout = ws + WS_H;
#pragma unroll
        for (int tb = 0; tb < 2; ++tb) {
            const int tl = 64 * wm + 32 * tb + r;
            const float mu = tmu[tl], rs = trs[tl], rs2 = rs * rs;
            char* rowp = hout + (size_t)(tm * 128 + tl) * (FF * 2) + tn * 192 + wn * 96 + h * 16;
#pragma unroll
            for (int j = 0; j < 3; ++j) {
                unsigned pk[2][2];
#pragma unroll
                for (int qd = 0; qd < 2; ++qd) {
                    const int fl = 16 * (3 * wn + j) + 8 * qd + 4 * h;
                    const f32x4 cg = *(LAS const f32x4*)(tcs + fl), cu = *(LAS const f32x4*)(tcs + 96 + fl);
                    float hv[4];
#pragma unroll
                    for (int i = 0; i < 4; ++i) {
                        const float g = acc[j][tb][4 * qd + i] - mu * cg[i];
                        const float u = acc[j][tb][8 + 4 * qd + i] - mu * cu[i];
                        hv[i] = fmaxf(g, 0.f) * (u * rs2);
                    }
                    pk[qd][0] = pack2(hv[0], hv[1]); pk[qd][1] = pack2(hv[2], hv[3]);
                }
                auto s0 = __builtin_amdgcn_permlane32_swap(pk[0][0], pk[1][0], false, false);
                auto s1 = __builtin_amdgcn_permlane32_swap(pk[0][1], pk[1][1], false, false);
                *(u32x4*)(rowp + j * 32) = (u32x4){s0[0], s1[0], s0[1], s1[1]};
            }
        }
    } else if (tn < 10) {
        STAMPW(1, 8); ln_mainloop<MODE, true>(smem, acc, wm, wn, lane, ta, tw, tws); STAMPW(2, 8);
        const bool isq = tn < 5;
        const float sc = isq ? QSCALE : 1.f;
#pragma unroll
        for (int tb = 0; tb < 2; ++tb) {
            const int tl = 64 * wm + 32 * tb + r, tok = tm * 128 + tl;
            const float mu = tmu[tl], rs = trs[tl] * sc;
#pragma unroll
            for (int cb = 0; cb < 2; ++cb)
#pragma unroll
                for (int qd = 0; qd < 4; ++qd) {
                    const int nl = 64 * wn + 32 * cb + 8 * qd + 4 * h;
                    const f32x4 cs = *(LAS const f32x4*)(tcs + nl);
                    float v[4];
#pragma unroll
                    for (int i = 0; i < 4; ++i) v[i] = rs * (acc[cb][tb][4 * qd + i] - mu * cs[i]);
                    const u32x2 pk = {pack2(v[0], v[1]), pack2(v[2], v[3])};
                    const int nn = (isq ? tn : tn - 5) * 128 + nl, head = nn / DH, d = nn - head * DH;
                    unsigned a;
                    if (isq) a = (unsigned)WS_Q + (head * SEQ + tok) * 40 + d * 2;
                    else {
                        const int kb = (tok >> 5) & 7;
                        a = (unsigned)WS_KV + (head * 8 + (tok >> 8)) * KVCH +
                            (d < 16 ? kb * 1024 + ((d >> 3) * 32 + r) * 16 + (d & 4) * 2 : KV_K1 + kb * 256 + r * 8);
                    }
                    *(u32x2*)(ws + a) = pk;
                }
        }
    } else {
        STAMPW(1, 8); ln_mainloop<MODE, false>(smem, acc, wm, wn, lane, ta, tw, tws); STAMPW(2, 8);
#pragma unroll
        for (int cb = 0; cb < 2; ++cb) {
            const int nl = 64 * wn + 32 * cb + r, nn = (tn - 10) * 128 + nl, head = nn / DH, d = nn - head * DH;
            const float cs = tcs[nl];
#pragma unroll
            for (int tb = 0; tb < 2; ++tb) {
                const int t0 = 64 * wm + 32 * tb, tok0 = tm * 128 + t0, kb = (tok0 >> 5) & 7;
                char* base = ws + ((unsigned)WS_KV + (head * 8 + (tok0 >> 8)) * KVCH + KV_V + 2 * kb * VSTEP + h * VHALF + d * 16);
#pragma unroll
                for (int s = 0; s < 2; ++s) {
                    u32x4 pk;
#pragma unroll
                    for (int qq = 0; qq < 2; ++qq) {
                        const int qd = 2 * s + qq, tl = t0 + 8 * qd + 4 * h;
                        const f32x4 mu = *(LAS const f32x4*)(tmu + tl), rs = *(LAS const f32x4*)(trs + tl);
                        float v[4];
#pragma unroll
                        for (int i = 0; i < 4; ++i) v[i] = rs[i] * (acc[cb][tb][4 * qd + i] - mu[i] * cs);
                        pk[2 * qq] = pack2(v[0], v[1]); pk[2 * qq + 1] = pack2(v[2], v[3]);
                    }
                    *(u32x4*)(base + s * VSTEP) = pk;
                    if (d >= DH - 2) { const unsigned cv = d == DH - 2 ? 0x3F803F80u : 0u; *(u32x4*)(base + s * VSTEP + 32) = (u32x4){cv, cv, cv, cv}; }
                }
            }
        }
    }
    STAMPW(3, 8);
}

DI float xsum16_32(float v) {
    auto a = __builtin_amdgcn_permlane16_swap(__float_as_uint(v), __float_as_uint(v), false, false);
    v = __uint_as_float(a[0]) + __uint_as_float(a[1]);
    auto b = __builtin_amdgcn_permlane32_swap(__float_as_uint(v), __float_as_uint(v), false, false);
    return __uint_as_float(b[0]) + __uint_as_float(b[1]);
}
template <int KT>
__global__ __launch_bounds__(768) void k_gemm_res(const char* __restrict__ A, const char* __restrict__ W, const char* __restrict__ res,
                                                  char* __restrict__ out, const char* __restrict__ wnext, char* __restrict__ ws DBG_ARG) {
    STAMP(0);
    constexpr int NS = KT / 64, NSLOT = 5, SLOT = 18432, DEP = 3;
    __shared__ __attribute__((aligned(1024))) char smem_[NSLOT * SLOT];
    LAS char* smem = (LAS char*)smem_;
    const int tid = threadIdx.x, lane = tid & 63, w = __builtin_amdgcn_readfirstlane(tid >> 6);
    const int xcd = blockIdx.x & 7, bi = blockIdx.x >> 3, pr = xcd >> 1, e = xcd & 1;
    const int tm = 8 * pr + (bi & 7), tn = 4 * e + (bi >> 3);

    if (w < 8) {
        const int j = w, row = j * 8 + (lane >> 3);
        const unsigned offa = (unsigned)((tm * 64 + row) * (KT * 2) + (((lane & 7) ^ ((row >> 1) & 7)) << 4));
        const unsigned offw = (unsigned)(tn * (NS * 10240) + j * 1024 + lane * 16);
        int kb = (NS / 2) * e, sl = 0;
        auto issue = [&]() {
            const unsigned sb = (unsigned)(size_t)smem + sl * SLOT + j * 1024;
            glds16s(A + kb * 128, offa, sb);
            glds16s(W + kb * 10240, offw, sb + 8192);
            if (j < 2) glds16s(W + kb * 10240 + 8192, offw, sb + 16384);
            kb = (kb == NS - 1) ? 0 : kb + 1; sl = (sl == NSLOT - 1) ? 0 : sl + 1;
        };
#define RS_WAIT(n) do { if (j < 2) WAITV(3 * (n)); else WAITV(2 * (n)); } while (0)
        issue(); issue(); issue();
        RS_WAIT(2);
        BAR_RAW();
#pragma unroll 1
        for (int s = 0; s < NS - DEP; ++s) {
            issue();
            RS_WAIT(2);
            BAR_RAW();
        }
        RS_WAIT(1);
        BAR_RAW();
        WAITV(0);
        BAR_RAW();
#undef RS_WAIT
        return;
    }

    const int cw = w - 8, r = lane & 15, g = lane >> 4;
    const int tok = tm * 64 + 16 * cw + r;
    unsigned tv = 0, tv2 = 0, tv3 = 0;
    constexpr int LPR = KT / 128;
    const char* ta = A + (size_t)(512 * pr + 16 * bi + 4 * cw + min(lane / LPR, 3)) * (KT * 2) + (1 - e) * KT + (lane % LPR) * 128;
    const unsigned tw0 = wqkv_line(bi, e, cw * 90 + min(lane, 44)), tw1 = wqkv_line(bi, e, cw * 90 + 45 + min(lane, 44));
    u32x2 rv[5];
#pragma unroll
    for (int j = 0; j < 5; ++j) rv[j] = *(const u32x2*)(res + (size_t)tok * 1280 + tn * 160 + j * 32 + g * 8);
    unsigned xa[2];
    { const int row = 16 * cw + r;
#pragma unroll
      for (int ks = 0; ks < 2; ++ks) xa[ks] = row * 128 + (((4 * ks + g) ^ ((row >> 1) & 7)) << 4); }
    const unsigned wa = 8192 + lane * 16;
    f32x4 acc[5];
#pragma unroll
    for (int j = 0; j < 5; ++j) acc[j] = (f32x4){0.f, 0.f, 0.f, 0.f};
    bf16x8 xf[2], wf[2][5];
    if (TOUCHA && KT == DM) tv = *(const unsigned*)ta;
    BAR_RAW();
    STAMPW(1, 8);
#pragma unroll
    for (int ks = 0; ks < 2; ++ks) {
        xf[ks] = lds128(smem + xa[ks]);
#pragma unroll
        for (int j = 0; j < 5; ++j) wf[ks][j] = lds128(smem + wa + ks * 5120 + j * 1024);
    }
    int slot = 0;
#pragma unroll 1
    for (int s = 0; s < NS - 1; ++s) {
        slot = (slot + 1 == NSLOT) ? 0 : slot + 1;
        LAS char* nb = smem + slot * SLOT;
        BAR_RAW();
        if (KT == FF) {
            if (TOUCHA && s == cw) tv = *(const unsigned*)ta;
            if (TOUCHW && s == 8 + cw) tv2 = *(const unsigned*)(wnext + tw0);
            if (TOUCHW && s == 13 + cw) tv3 = *(const unsigned*)(wnext + tw1);
        }
        __builtin_amdgcn_sched_barrier(0);
#pragma unroll
        for (int ks = 0; ks < 2; ++ks) {
#pragma unroll
            for (int j = 0; j < 5; ++j) {
                acc[j] = MFMA16(wf[ks][j], xf[ks], acc[j]);
                wf[ks][j] = lds128(nb + wa + ks * 5120 + j * 1024);
                if (j == 4) xf[ks] = lds128(nb + xa[ks]);
                __builtin_amdgcn_sched_barrier(0);
            }
        }
    }
#pragma unroll
    for (int ks = 0; ks < 2; ++ks)
#pragma unroll
        for (int j = 0; j < 5; ++j) acc[j] = MFMA16(wf[ks][j], xf[ks], acc[j]);
    STAMPW(2, 8);
    float v[20]; float sum = 0.f;
#pragma unroll
    for (int j = 0; j < 5; ++j) {
        const u32x2 pk = {pack2(acc[j][0] + bflo(rv[j][0]), acc[j][1] + bfhi(rv[j][0])), pack2(acc[j][2] + bflo(rv[j][1]), acc[j][3] + bfhi(rv[j][1]))};
        *(u32x2*)(out + (size_t)tok * 1280 + tn * 160 + j * 32 + g * 8) = pk;
        v[4 * j] = bflo(pk[0]); v[4 * j + 1] = bfhi(pk[0]); v[4 * j + 2] = bflo(pk[1]); v[4 * j + 3] = bfhi(pk[1]);
        sum += (v[4 * j] + v[4 * j + 1]) + (v[4 * j + 2] + v[4 * j + 3]);
    }
    const float mean = xsum16_32(sum) * (1.f / 80.f);
    float m2 = 0.f;
#pragma unroll
    for (int i = 0; i < 20; ++i) { const float d = v[i] - mean; m2 += d * d; }
    m2 = xsum16_32(m2);
    if (g == 0) *(f32x2*)(ws + WS_ST + ((size_t)tok * 8 + tn) * 8) = (f32x2){mean, m2};
    asm volatile("" ::"v"(tv), "v"(tv2), "v"(tv3));
    STAMPW(3, 8);
}

DI void attn_store(char* O, int tok, int head, int h, const f32x16& o, float inv) {
    char* p = O + (size_t)tok * 1280 + head * 40 + h * 8;
    *(u32x2*)(p) = (u32x2){pack2(o[0] * inv, o[1] * inv), pack2(o[2] * inv, o[3] * inv)};
    *(u32x2*)(p + 16) = (u32x2){pack2(o[4] * inv, o[5] * inv), pack2(o[6] * inv, o[7] * inv)};
    if (h == 0) *(u32x2*)(p + 32) = (u32x2){pack2(o[8] * inv, o[9] * inv), pack2(o[10] * inv, o[11] * inv)};
}
DI float swap_max(float m) { auto rr = __builtin_amdgcn_permlane32_swap(__float_as_uint(m), __float_as_uint(m), false, false); return fmaxf(__uint_as_float(rr[0]), __uint_as_float(rr[1])); }
DI float max16(const f32x16& s) {
    float a = fmaxf(fmaxf(s[0], s[1]), s[2]), b = fmaxf(fmaxf(s[3], s[4]), s[5]);
    a = fmaxf(fmaxf(a, s[6]), s[7]); b = fmaxf(fmaxf(b, s[8]), s[9]);
    a = fmaxf(fmaxf(a, s[10]), s[11]); b = fmaxf(fmaxf(b, s[12]), s[13]);
    a = fmaxf(fmaxf(a, s[14]), s[15]);
    return swap_max(fmaxf(a, b));
}
DI bf16x8 packp(const f32x16& p, int s) {
    u32x4 u = {pack2(p[8 * s], p[8 * s + 1]), pack2(p[8 * s + 2], p[8 * s + 3]), pack2(p[8 * s + 4], p[8 * s + 5]), pack2(p[8 * s + 6], p[8 * s + 7])};
    return __builtin_bit_cast(bf16x8, u);
}

__global__ __launch_bounds__(1024) void k_attn(char* __restrict__ ws DBG_ARG) {
    STAMP(0);
    constexpr int NSL = 7, QOFF = NSL * KVCH;
    __shared__ __attribute__((aligned(1024))) char smem_[QOFF + 10240];
    LAS char* smem = (LAS char*)smem_;
    const int tid = threadIdx.x, lane = tid & 63, w = __builtin_amdgcn_readfirstlane(tid >> 6);
    const int xcd = blockIdx.x & 7, bi = blockIdx.x >> 3, pr = xcd >> 1, e = xcd & 1;
    const int head = 16 * e + (bi >> 1), qt = 2 * pr + (bi & 1);
    const char* kv = ws + WS_KV + (size_t)head * 8 * KVCH;
    const int r = lane & 31, h = lane >> 5, qb = w & 7, kh = w >> 3;

    const unsigned l16 = lane * 16, sm0 = (unsigned)(size_t)smem;
    const char* kv0 = kv + 2 * pr * KVCH;
    glds16s(kv0 + w * 1024, l16, sm0 + w * 1024);
    if (w < 10) glds16s(ws + WS_Q + (size_t)(head * SEQ + qt * 256) * 40 + w * 1024, l16, sm0 + QOFF + w * 1024);
    else glds16s(kv0 + (w + 6) * 1024, l16, sm0 + (w + 6) * 1024);
    glds16s(kv0 + (w + 22) * 1024, l16, sm0 + (w + 22) * 1024);
    if (w < 4) { glds16s(kv0 + (w + 38) * 1024, l16, sm0 + (w + 38) * 1024); WAITV(2); } else WAITV(1);
    BAR_RAW();
    STAMP(1);

    bf16x8 q0; bf16x4 q1;
    { LAS const char* qp = smem + QOFF + (qb * 32 + r) * 40;
      q0 = cat(lds64(qp + h * 16), lds64(qp + h * 16 + 8));
      q1 = lds64(qp + 32);
      if (h) q1 = (bf16x4){0, 0, 0, 0}; }
    const unsigned k0a = lane * 16, k1a = KV_K1 + r * 8;
    const unsigned va = KV_V + h * VHALF + min(r, 21) * 16;
    f32x16 zero, negm, o;
#pragma unroll
    for (int i = 0; i < 16; ++i) { zero[i] = 0.f; o[i] = 0.f; }
    {
        f32x16 s = MFMA32(lds128(smem + k0a), q0, zero);
        s = MFMA32K8(lds64(smem + k1a), q1, s);
        const float m = max16(s);
#pragma unroll
        for (int i = 0; i < 16; ++i) negm[i] = -m;
    }
    LAS const char* pk0 = smem + k0a + kh * 4096;
    LAS const char* pk1 = smem + k1a + kh * 1024;
    LAS const char* pv = smem + va + kh * 8 * VSTEP;
#define PIN(x) asm volatile("" : "+v"(x))
#define SBAR() __builtin_amdgcn_sched_barrier(0)
#define EX(x) __builtin_amdgcn_exp2f(x)
#define ATT_STEP(SN, SC, PK0, PK1, PV0, PV1) do { \
        float t0, t1, t2, t3; \
        SN = MFMA32(ka, q0, negm); PIN(SN); ka = lds128(PK0); \
        pw1[2] = pack2(e0, e1); pw1[3] = pack2(e2, e3); PIN(pw1); SBAR(); \
        t0 = EX(SC[0]); t1 = EX(SC[1]); t2 = EX(SC[2]); t3 = EX(SC[3]); PIN(t0); PIN(t1); PIN(t2); PIN(t3); SBAR(); \
        o = MFMA32(v1, __builtin_bit_cast(bf16x8, pw1), o); PIN(o); v1 = lds128(PV1); \
        pw0[0] = pack2(t0, t1); pw0[1] = pack2(t2, t3); PIN(pw0); SBAR(); \
        t0 = EX(SC[4]); t1 = EX(SC[5]); t2 = EX(SC[6]); t3 = EX(SC[7]); PIN(t0); PIN(t1); PIN(t2); PIN(t3); SBAR(); \
        SN = MFMA32K8(kb, q1, SN); PIN(SN); kb = lds64(PK1); \
        pw0[2] = pack2(t0, t1); pw0[3] = pack2(t2, t3); PIN(pw0); SBAR(); \
        t0 = EX(SC[8]); t1 = EX(SC[9]); t2 = EX(SC[10]); t3 = EX(SC[11]); PIN(t0); PIN(t1); PIN(t2); PIN(t3); SBAR(); \
        o = MFMA32(v0, __builtin_bit_cast(bf16x8, pw0), o); PIN(o); v0 = lds128(PV0); \
        pw1[0] = pack2(t0, t1); pw1[1] = pack2(t2, t3); PIN(pw1); SBAR(); \
        e0 = EX(SC[12]); e1 = EX(SC[13]); e2 = EX(SC[14]); e3 = EX(SC[15]); PIN(e0); PIN(e1); PIN(e2); PIN(e3); SBAR(); \
    } while (0)
#define ATT_LOAD() do { if (nxt < 168) glds16s(kv + (nxt + 42 * pr >= 168 ? nxt + 42 * pr - 168 : nxt + 42 * pr) * 1024, l16, sm0 + (nxt >= 147 ? nxt - 147 : nxt) * 1024); nxt += 16; } while (0)
#define ATT_CHUNK(LD, SYNC, STEP_) do { \
        LAS const char* nk0 = pk0 + (STEP_); LAS const char* nk1 = pk1 + (STEP_); LAS const char* nv = pv + (STEP_); \
        LD; ATT_STEP(sB, sA, pk0 + 2048, pk1 + 512, pv + 2 * VSTEP, pv + VSTEP); \
        LD; ATT_STEP(sA, sB, pk0 + 3072, pk1 + 768, pv + 4 * VSTEP, pv + 3 * VSTEP); \
        SYNC; \
        LD; ATT_STEP(sB, sA, nk0, nk1, pv + 6 * VSTEP, pv + 5 * VSTEP); \
        LD; ATT_STEP(sA, sB, nk0 + 1024, nk1 + 256, nv, pv + 7 * VSTEP); \
        pk0 = nk0; pk1 = nk1; pv = nv; \
    } while (0)
#define ATS(i)
#define PRIO(n) __builtin_amdgcn_s_setprio(n)
    unsigned tv = 0;
    int nxt = 42 + w;
    f32x16 sA = MFMA32(lds128(pk0), q0, negm), sB;
    sA = MFMA32K8(lds64(pk1), q1, sA);
    bf16x8 ka = lds128(pk0 + 1024), v0 = lds128(pv), v1 = {0, 0, 0, 0, 0, 0, 0, 0};
    bf16x4 kb = lds64(pk1 + 256);
    u32x4 pw0 = {0, 0, 0, 0}, pw1 = {0, 0, 0, 0};
    float e0 = 0.f, e1 = 0.f, e2 = 0.f, e3 = 0.f;
    ATS(9);
#pragma unroll 1
    for (int c = 0; c < 2; ++c)
        ATT_CHUNK(ATT_LOAD(), do { if (c == 0) ATS(0); else ATS(2); WAITV(2); BAR_RAW(); if (c == 0) ATS(1); else ATS(3); PRIO(3); } while (0), KVCH);
#pragma unroll 1
    for (int c = 2; c < 8; ++c) {
        if (c == 2 || c == 5) PRIO(2); else if (c == 3 || c == 6) PRIO(1); else PRIO(0);
        ATT_CHUNK((void)0, do { if (c == 4) {
                ATS(4); WAITV(0); BAR_RAW(); ATS(5); PRIO(3);
                if (tid < 100) tv = *(const unsigned*)(ws + WS_WO + (size_t)e * 409600 + bi * 12800 + tid * 128);
                else if (tid >= 128 && tid < 608) tv = *(const unsigned*)(ws + WS_WGU + (size_t)e * 1966080 + bi * 61440 + (tid - 128) * 128);
            } } while (0), (c == 6) ? -6 * KVCH : KVCH);
    }
    pw1[2] = pack2(e0, e1); pw1[3] = pack2(e2, e3);
    o = MFMA32(v1, __builtin_bit_cast(bf16x8, pw1), o);
    asm volatile("" ::"v"(tv));
    ATS(6);
#undef ATT_CHUNK
#undef ATT_LOAD
    STAMP(2);
#undef ATT_STEP
    LAS f32x4* cb = (LAS f32x4*)(smem + KVCH) + qb * 192 + lane;
    if (kh == 1) {
#pragma unroll
        for (int i = 0; i < 3; ++i) cb[i * 64] = (f32x4){o[4 * i], o[4 * i + 1], o[4 * i + 2], o[4 * i + 3]};
    }
    BAR_LGKM();
    if (kh == 1) return;
#pragma unroll
    for (int i = 0; i < 3; ++i) { const f32x4 t = cb[i * 64]; o[4 * i] += t[0]; o[4 * i + 1] += t[1]; o[4 * i + 2] += t[2]; o[4 * i + 3] += t[3]; }
    float l;
    { auto rr = __builtin_amdgcn_permlane32_swap(__float_as_uint(o[8]), __float_as_uint(o[8]), false, false); l = __uint_as_float(rr[1]); }
    float chk = 0.f;
#pragma unroll
    for (int i = 0; i < 12; ++i) chk += o[i] * 0.f;
    const bool ok = (chk == 0.f) && (l <= 1.8446744e19f);
    const int tok = qt * 256 + qb * 32 + r;
    if (__builtin_expect(!__all(ok), 0)) {
        float m = -INFINITY;
#pragma unroll
        for (int i = 0; i < 16; ++i) o[i] = 0.f;
#pragma unroll 1
        for (int kb = 0; kb < 64; ++kb) {
            const char* cp = kv + (size_t)(kb >> 3) * KVCH; const int u = kb & 7;
            f32x16 s = MFMA32(*(const bf16x8*)(cp + k0a + u * 1024), q0, zero);
            s = MFMA32K8(*(const bf16x4*)(cp + k1a + u * 256), q1, s);
            const float mn = fmaxf(m, max16(s));
            const float al = __builtin_amdgcn_exp2f(m - mn);
            m = mn;
#pragma unroll
            for (int i = 0; i < 16; ++i) { o[i] *= al; s[i] = __builtin_amdgcn_exp2f(s[i] - mn); }
            o = MFMA32(*(const bf16x8*)(cp + va + (2 * u) * VSTEP), packp(s, 0), o);
            o = MFMA32(*(const bf16x8*)(cp + va + (2 * u + 1) * VSTEP), packp(s, 1), o);
        }
        auto rr = __builtin_amdgcn_permlane32_swap(__float_as_uint(o[8]), __float_as_uint(o[8]), false, false); l = __uint_as_float(rr[1]);
    }
    attn_store(ws + WS_O, tok, head, h, o, 1.f / l);
    STAMP(3);
}

extern "C" void kernel_launch(void* const* d_in, const int* in_sizes, int n_in, void* d_out, int out_size, void* d_ws, size_t ws_size,
                              hipStream_t stream) {
    const char* x0 = (const char*)d_in[0];
    const char* wqkv = (const char*)d_in[1];
    const char* wo = (const char*)d_in[2];
    const char* wg = (const char*)d_in[4];
    const char* wu = (const char*)d_in[5];
    const char* wd = (const char*)d_in[6];
    char* ws = (char*)d_ws;
    char* out = (char*)d_out;
    k_prep<<<256, 896, 0, stream>>>(x0, wqkv, wo, wg, wu, wd, ws DBG_PASS(0));
    for (int layer = 0; layer < 2; ++layer) {
        const char* xin = layer == 0 ? x0 : ws + WS_X2;
        char* xa = ws + (layer == 0 ? WS_X1 : WS_X3);
        char* xb = layer == 0 ? ws + WS_X2 : out;
        k_gemm_ln<0><<<240, 768, 0, stream>>>(xin, wqkv, ws DBG_PASS(1 + 5 * layer));
        k_attn<<<256, 1024, 0, stream>>>(ws DBG_PASS(2 + 5 * layer));
        k_gemm_res<DM><<<256, 768, 0, stream>>>(ws + WS_O, ws + WS_WO, xin, xa, wqkv, ws DBG_PASS(3 + 5 * layer));
        k_gemm_ln<1><<<256, 768, 0, stream>>>(xa, ws + WS_WGU, ws DBG_PASS(4 + 5 * layer));
        k_gemm_res<FF><<<256, 768, 0, stream>>>(ws + WS_H, ws + WS_WD, xa, xb, wqkv, ws DBG_PASS(5 + 5 * layer));
    }
}
```

```cpp
#include <hip/hip_runtime.h>
#include <hip/hip_bf16.h>

typedef short bf16x8 __attribute__((ext_vector_type(8)));
typedef short bf16x4 __attribute__((ext_vector_type(4)));
typedef float f32x16 __attribute__((ext_vector_type(16)));
typedef float f32x4 __attribute__((ext_vector_type(4)));
typedef float f32x2 __attribute__((ext_vector_type(2)));
typedef unsigned u32x4 __attribute__((ext_vector_type(4)));
typedef unsigned u32x2 __attribute__((ext_vector_type(2)));
typedef __bf16 bf2_t __attribute__((ext_vector_type(2)));
#define LAS __attribute__((address_space(3)))
#define DI __device__ __forceinline__
#define MFMA32(a, b, c) __builtin_amdgcn_mfma_f32_32x32x16_bf16((a), (b), (c), 0, 0, 0)
#define MFMA32K8(a, b, c) __builtin_amdgcn_mfma_f32_32x32x8bf16_1k((a), (b), (c), 0, 0, 0)
#define MFMA4(a, b, c) __builtin_amdgcn_mfma_f32_4x4x4bf16_1k((a), (b), (c), 0, 0, 0)
#define MFMA16(a, b, c) __builtin_amdgcn_mfma_f32_16x16x32_bf16((a), (b), (c), 0, 0, 0)
#define WAITV(n) asm volatile("s_waitcnt vmcnt(%0)" ::"i"(n) : "memory")
#define BAR_LGKM() asm volatile("s_waitcnt lgkmcnt(0)\n\ts_barrier" ::: "memory")
#define BAR_RAW() asm volatile("s_barrier" ::: "memory")
#define PIN(x) asm volatile("" : "+v"(x))
#define SBAR() __builtin_amdgcn_sched_barrier(0)

constexpr int SEQ = 2048, DM = 640, NH = 32, DH = 20, FF = 1536;
constexpr float QSCALE = 0.22360679774997896f * 1.4426950408889634f;
constexpr float LN_EPS = 1e-5f;
constexpr int LDS_BYTES = 163840;

constexpr unsigned SZ_X = SEQ * DM * 2, SZ_ST = 32 * 8 * 512;
constexpr unsigned WS_XA = 0;
constexpr unsigned WS_XB = WS_XA + 2 * SZ_X;
constexpr unsigned WS_O = WS_XB + SZ_X;
constexpr unsigned WS_V = WS_O + 2 * SZ_X;
constexpr unsigned WS_QK = WS_V + 2 * SZ_X;
constexpr unsigned WS_H = WS_QK + 4 * SZ_X;
constexpr unsigned WS_ST = WS_H + 2 * SEQ * FF * 2;
constexpr unsigned WS_WO = WS_ST + 3 * SZ_ST;
constexpr unsigned WS_WD = WS_WO + DM * DM * 2;
constexpr unsigned WS_WGU = WS_WD + FF * DM * 2;
constexpr unsigned WS_CSQ = WS_WGU + 2 * DM * FF * 2;
constexpr unsigned WS_CSGU = WS_CSQ + 8192;

#define DBG_ARG
#define DBG_PASS(k)
#define STAMPW(i, wv)

#define EXP_ROT
#define NOSTAT 0
#define NOSLAB 0
DI unsigned pack2(float a, float b) { f32x2 v = {a, b}; return __builtin_bit_cast(unsigned, __builtin_convertvector(v, bf2_t)); }
DI float bflo(unsigned u) { return __uint_as_float(u << 16); }
DI float bfhi(unsigned u) { return __uint_as_float(u & 0xffff0000u); }
DI void glds16s(const char* sb_, unsigned voff, unsigned ldsaddr) {
    const unsigned long long sbase = ((unsigned long long)(unsigned)__builtin_amdgcn_readfirstlane((int)((unsigned long long)sb_ >> 32)) << 32) |
                                     (unsigned)__builtin_amdgcn_readfirstlane((int)(unsigned long long)sb_);
    ldsaddr = __builtin_amdgcn_readfirstlane(ldsaddr);
    asm volatile("s_mov_b32 m0, %2\n\ts_nop 3\n\tglobal_load_lds_dwordx4 %1, %0" ::"s"(sbase), "v"(voff), "s"(ldsaddr) : "memory");
}
DI void st128(char* p, u32x4 v) { *(u32x4*)p = v; }
DI void st64(char* p, u32x2 v) { *(u32x2*)p = v; }
DI void st128s(char* sb_, unsigned voff, u32x4 v) {
    const unsigned long long sbase = ((unsigned long long)(unsigned)__builtin_amdgcn_readfirstlane((int)((unsigned long long)sb_ >> 32)) << 32) |
                                     (unsigned)__builtin_amdgcn_readfirstlane((int)(unsigned long long)sb_);
    asm volatile("s_nop 4\n\tglobal_store_dwordx4 %1, %2, %0 sc1\n\ts_nop 1" ::"s"(sbase), "v"(voff), "v"(v) : "memory");
}
DI bf16x8 lds128(LAS const char* p) { return *(LAS const bf16x8*)p; }
DI bf16x4 lds64(LAS const char* p) { return *(LAS const bf16x4*)p; }
DI bf16x4 ldstr(LAS const char* p) { return __builtin_bit_cast(bf16x4, __builtin_amdgcn_ds_read_tr16_b64_v4i16((LAS bf16x4*)p)); }
DI bf16x8 cat(bf16x4 a, bf16x4 b) { return __builtin_shufflevector(a, b, 0, 1, 2, 3, 4, 5, 6, 7); }
DI float half_sum(float v) { auto a = __builtin_amdgcn_permlane32_swap(__float_as_uint(v), __float_as_uint(v), false, false); return __uint_as_float(a[0]) + __uint_as_float(a[1]); }
DI float xsum16_32(float v) {
    auto a = __builtin_amdgcn_permlane16_swap(__float_as_uint(v), __float_as_uint(v), false, false);
    return half_sum(__uint_as_float(a[0]) + __uint_as_float(a[1]));
}
DI u32x4 split3(float v, bool rowside) {
    const unsigned p1 = pack2(v, v); const float r1 = v - bflo(p1);
    const unsigned p2 = pack2(r1, r1); const float r2 = r1 - bflo(p2);
    const unsigned p3 = pack2(r2, r2);
    const unsigned l1 = p1 & 0xffffu, l2 = p2 & 0xffffu, h1 = p1 & 0xffff0000u, h2 = p2 & 0xffff0000u, h3 = p3 & 0xffff0000u;
    return rowside ? (u32x4){p1, l2 | h1, l2 | h3, 0u} : (u32x4){l1 | h2, l1 | h3, l2 | h1, 0u};
}
DI bf16x4 lo4(bf16x8 v) { return __builtin_shufflevector(v, v, 0, 1, 2, 3); }
DI bf16x4 hi4(bf16x8 v) { return __builtin_shufflevector(v, v, 4, 5, 6, 7); }

template <int MODE> struct LnCfg;
template <> struct LnCfg<0> { static constexpr int SLOT = 32768, NB = 2, PPW = 4, NCS = 128; };
template <> struct LnCfg<1> { static constexpr int SLOT = 40960, NB = 3, PPW = 5, NCS = 192; };
constexpr int T_XC = 0, T_WC = 2048, T_RS = 5120, T_RAW = 5632;

template <int MODE, bool FIRST, bool SWAP>
DI void ln_mainloop(LAS char* smem, f32x16 (&acc)[LnCfg<MODE>::NB][2], int wm, int wn, int lane, char* ws, int tm, int tn) {
    using C = LnCfg<MODE>;
    constexpr int NB = C::NB;
    const int r = lane & 31, h = lane >> 5, q = (lane & 15) >> 2, pp = lane & 3, blk = (lane >> 4) & 1;
    unsigned xa[4], wa[2];
    if (FIRST) {
        const int row = 64 * wm + 32 * wn + r, sw = (row >> 1) & 7;
#pragma unroll
        for (int ks = 0; ks < 4; ++ks) xa[ks] = row * 128 + (((2 * ks + h) ^ sw) << 4);
    } else xa[0] = wm * 8192 + h * 1024 + r * 16;
    if (MODE == 0) {
#pragma unroll
        for (int cb = 0; cb < 2; ++cb) wa[cb] = 16384 + (8 * h + q) * 256 + (((2 * wn + (FIRST ? cb ^ wm : cb)) ^ q) * 64) + blk * 32 + pp * 8;
    } else { wa[0] = 16384 + wn * 3072 + lane * 16; wa[1] = 0; }
    bf16x8 xf[2][2], wf[2][NB];
    f32x4 sx = {0.f, 0.f, 0.f, 0.f}, sw_ = sx; f32x16 sxx;
#pragma unroll
    for (int i = 0; i < 16; ++i) sxx[i] = 0.f;
    const short sv = ((lane & 15) == ((lane >> 4) & 1)) ? (short)0x3F80 : (short)0;
    const bf16x8 sel = {sv, sv, sv, sv, sv, sv, sv, sv};
#define LN_READ(BUF, SB, KS) do { \
        _Pragma("unroll") for (int tb = 0; tb < 2; ++tb) xf[BUF][tb] = FIRST ? lds128((SB) + (xa[KS] ^ (tb * 4096))) : lds128((SB) + xa[0] + (KS) * 2048 + tb * 512); \
        _Pragma("unroll") for (int j = 0; j < NB; ++j) { \
            if (MODE == 0) wf[BUF][j] = cat(ldstr((SB) + wa[j] + (KS) * 4096), ldstr((SB) + wa[j] + (KS) * 4096 + 1024)); \
            else wf[BUF][j] = lds128((SB) + wa[0] + (KS) * 6144 + j * 1024); } \
    } while (0)
#define LN_MMA(BUF) do { \
        _Pragma("unroll") for (int j = 0; j < NB; ++j) _Pragma("unroll") for (int tb = 0; tb < 2; ++tb) \
            acc[j][tb] = SWAP ? MFMA32(wf[BUF][j], xf[BUF][tb], acc[j][tb]) : MFMA32(xf[BUF][tb], wf[BUF][j], acc[j][tb]); \
        if (FIRST && !NOSTAT) { sx = MFMA16(sel, xf[BUF][0], sx); sw_ = MFMA16(sel, wf[BUF][0], sw_); sxx = MFMA32(xf[BUF][0], xf[BUF][0], sxx); } \
    } while (0)
#define LN_STEP(RB, SB, KS, MB) do { LN_READ(RB, SB, KS); LN_MMA(MB); \
        _Pragma("unroll") for (int i_ = 0; i_ < 3; ++i_) { __builtin_amdgcn_sched_group_barrier(0x8, 1, 0); __builtin_amdgcn_sched_group_barrier(0x100, 2, 0); } \
        __builtin_amdgcn_sched_group_barrier(0x8, 2 * NB - 3 + (FIRST ? 3 : 0), 0); \
        SBAR(); } while (0)
    BAR_RAW();
    LN_READ(0, smem, 0);
#pragma unroll 1
    for (int s = 0; s < 9; ++s) {
        LAS char* sb = smem + (s & 3) * C::SLOT;
        LAS char* nb = smem + ((s + 1) & 3) * C::SLOT;
        SBAR();
        LN_STEP(1, sb, 1, 0);
        LN_STEP(0, sb, 2, 1);
        LN_STEP(1, sb, 3, 0);
        BAR_RAW();
        SBAR();
        LN_STEP(0, nb, 0, 1);
    }
    LAS char* tab = smem + 3 * C::SLOT;
    {
        LAS char* sb = smem + C::SLOT;
        SBAR();
        LN_STEP(1, sb, 1, 0);
        LN_STEP(0, sb, 2, 1);
        LN_STEP(1, sb, 3, 0);
        if (!FIRST) BAR_RAW();
        LN_MMA(1);
    }
    if (FIRST) {
        LAS float* raw = (LAS float*)(tab + T_RAW) + (wn * 2 + wm) * 96;
        if (lane < 16) { raw[lane] = sx[0]; raw[16 + lane] = sx[1]; raw[32 + lane] = sw_[0]; raw[48 + lane] = sw_[1]; }
        { const int ri = (r & 3) + 4 * (r >> 3); float dg = sxx[0];
#pragma unroll
          for (int i = 1; i < 16; ++i) dg = ri == i ? sxx[i] : dg;
          if (((r >> 2) & 1) == h) raw[64 + r] = dg; }
        asm volatile("s_waitcnt lgkmcnt(0)" ::: "memory");
        const float csum = raw[32 + r];
        const float mu = raw[r] * (1.f / DM), var = fmaxf(raw[64 + r] * (1.f / DM) - mu * mu, 0.f);
        const int tl = 64 * wm + 32 * wn + r, nl = 64 * wn + 32 * wm + r;
        if (h == 0) { *(LAS u32x4*)(tab + T_XC + tl * 16) = split3(-mu, true); *(LAS float*)(tab + T_RS + tl * 4) = __builtin_amdgcn_rsqf(var + LN_EPS); }
        else { *(LAS u32x4*)(tab + T_WC + nl * 16) = split3(csum, false); if (tm == 0) *(float*)(ws + WS_CSQ + (tn * 128 + nl) * 4) = csum; }
        BAR_LGKM();
    }
    {
        bf16x4 xc[2], wc[NB];
#pragma unroll
        for (int tb = 0; tb < 2; ++tb) xc[tb] = lds64(tab + T_XC + (64 * wm + 32 * (FIRST ? tb ^ wn : tb) + r) * 16 + h * 8);
#pragma unroll
        for (int j = 0; j < NB; ++j) wc[j] = lds64(tab + T_WC + ((NB * wn + (FIRST ? j ^ wm : j)) * 32 + r) * 16 + h * 8);
#pragma unroll
        for (int j = 0; j < NB; ++j)
#pragma unroll
            for (int tb = 0; tb < 2; ++tb) acc[j][tb] = SWAP ? MFMA32K8(wc[j], xc[tb], acc[j][tb]) : MFMA32K8(xc[tb], wc[j], acc[j][tb]);
    }
#undef LN_STEP
#undef LN_READ
#undef LN_MMA
}

struct Wts { const char *wo, *wg, *wu, *wd; };

template <int MODE, bool FIRST>
DI void phase_ln(LAS char* smem, const char* __restrict__ X, const char* __restrict__ W0, const char* __restrict__ stats, char* __restrict__ out0,
                 char* __restrict__ out1, char* __restrict__ ws, const Wts& wt DBG_ARG) {
    using C = LnCfg<MODE>;
    const int tid = threadIdx.x, lane = tid & 63, w = __builtin_amdgcn_readfirstlane(tid >> 6);
    const int xcd = blockIdx.x & 7, bi = blockIdx.x >> 3, pr = xcd >> 1, e = xcd & 1;
    const unsigned sm0 = (unsigned)(size_t)smem;
    int tm, tn;
    if (MODE == 0) { const int lin = e * 30 + bi; tm = 4 * pr + (lin & 3); tn = lin >> 2; }
    else { tm = 4 * pr + (bi & 3); tn = 8 * e + (bi >> 2); }
    LAS char* tab = smem + 3 * C::SLOT;

    if (MODE == 0 && bi >= 30) {
        if (FIRST) {
            const int blk = (xcd * 2 + bi - 30) * 3 + (tid >> 8), m = blk / 24, c = tid & 7, rg = (tid & 255) >> 3;
            const char* p = (m ? wt.wu : wt.wg) + rg * 3072 + (blk - 24 * m) * 128 + c * 16;
            float s[8];
#pragma unroll
            for (int i = 0; i < 8; ++i) s[i] = 0.f;
            u32x4 v[20];
#pragma unroll
            for (int j = 0; j < 20; ++j) v[j] = *(const u32x4*)(p + j * 32 * 3072);
#pragma unroll
            for (int j = 0; j < 20; ++j)
#pragma unroll
                for (int i = 0; i < 4; ++i) { s[2 * i] += bflo(v[j][i]); s[2 * i + 1] += bfhi(v[j][i]); }
            LAS float* red = (LAS float*)smem;
#pragma unroll
            for (int i = 0; i < 8; ++i) red[rg * 192 + (tid >> 8) * 64 + c * 8 + i] = s[i];
            BAR_LGKM();
            if (tid < 192) {
                float t = 0.f;
#pragma unroll
                for (int i = 0; i < 32; ++i) t += red[i * 192 + tid];
                const int b2 = (xcd * 2 + bi - 30) * 3 + (tid >> 6), m2 = b2 / 24, f = (b2 - 24 * m2) * 64 + (tid & 63), tf = f / 96, fl = f - tf * 96;
                *(float*)(ws + WS_CSGU + (tf * 192 + (fl >> 4) * 32 + m2 * 16 + (fl & 15)) * 4) = t;
            }
        }
        return;
    }

    if (w < 8) {
        const int j = w;
        unsigned off[C::PPW];
#pragma unroll
        for (int p = 0; p < 2; ++p) {
            if (FIRST) { const int row = (j + 8 * p) * 8 + (lane >> 3); off[p] = (unsigned)((tm * 128 + row) * 1280 + (((lane & 7) ^ ((row >> 1) & 7)) << 4)); }
            else off[p] = (unsigned)((2 * tm + p) * 81920 + j * 1024 + lane * 16);
        }
#pragma unroll
        for (int p = 2; p < C::PPW; ++p) {
            const int piece = j + 8 * (p - 2);
            if (MODE == 0) { const int k = piece * 4 + (lane >> 4), g = lane & 15;
                off[p] = (unsigned)(k * 3840 + tn * 256 + (((g >> 2) ^ (k & 3)) << 6) + (g & 3) * 16); }
            else off[p] = (unsigned)(tn * 245760 + piece * 1024 + lane * 16);
        }
        constexpr int XSTAGE = FIRST ? 128 : 8192, WSTAGE = MODE == 0 ? 64 * 3840 : 24576;
        int kb = FIRST ? (3 * (tm & 3) + 7 * tn) % 10 : 0;
        auto issue = [&](int s) {
            const unsigned sb = sm0 + (s & 3) * C::SLOT + j * 1024;
#pragma unroll
            for (int p = 0; p < C::PPW; ++p) glds16s(p < 2 ? X + kb * XSTAGE : W0 + kb * WSTAGE, off[p], sb + p * 8192);
            kb = (kb == 9) ? 0 : kb + 1;
        };
        issue(0); issue(1);
        if (FIRST) {
            WAITV(C::PPW);
            BAR_RAW();
#pragma unroll 1
            for (int s = 0; s < 8; ++s) { issue(s + 2); WAITV(C::PPW); BAR_RAW(); }
            WAITV(0);
            BAR_RAW();
            BAR_RAW();
        } else {
            asm volatile("" ::: "memory");
            const char* sp = stats + (2 * tm + (tid >> 8)) * 4096 + (tid & 3) * 1024 + ((tid >> 2) & 63) * 8;
            const f32x2 st0 = *(const f32x2*)sp, st1 = *(const f32x2*)(sp + 512);
            const float cs = *(const float*)(ws + (MODE == 0 ? WS_CSQ : WS_CSGU) + (tn * C::NCS + (tid < C::NCS ? tid : 0)) * 4);
            WAITV(C::PPW + 3);
            BAR_RAW();
#pragma unroll 1
            for (int s = 0; s < 8; ++s) { issue(s + 2); WAITV(C::PPW); BAR_RAW(); }
            WAITV(0);
            BAR_RAW();
            float sm = st0[0] + st1[0];
            sm += __shfl_xor(sm, 1); sm += __shfl_xor(sm, 2);
            const float mu = sm * 0.125f, d0 = st0[0] - mu, d1 = st1[0] - mu;
            float m2 = st0[1] + st1[1] + 80.f * (d0 * d0 + d1 * d1);
            m2 += __shfl_xor(m2, 1); m2 += __shfl_xor(m2, 2);
            if ((tid & 3) == 0) { *(LAS u32x4*)(tab + T_XC + (tid >> 2) * 16) = split3(-mu, true);
                                  *(LAS float*)(tab + T_RS + (tid >> 2) * 4) = __builtin_amdgcn_rsqf(m2 * (1.f / DM) + LN_EPS); }
            if (tid < C::NCS) *(LAS u32x4*)(tab + T_WC + tid * 16) = split3(cs, false);
            BAR_LGKM();
        }
        return;
    }

    const int wm = w & 1, wn = (w >> 1) & 1, r = lane & 31, h = lane >> 5;
    LAS const float* trs = (LAS const float*)(tab + T_RS);
    f32x16 acc[C::NB][2];
#pragma unroll
    for (int j = 0; j < C::NB; ++j)
#pragma unroll
        for (int tb = 0; tb < 2; ++tb)
#pragma unroll
            for (int i = 0; i < 16; ++i) acc[j][tb][i] = 0.f;

    if (MODE == 1) {
        STAMPW(1, 8); ln_mainloop<MODE, FIRST, true>(smem, acc, wm, wn, lane, ws, tm, tn); STAMPW(2, 8);
        char* hp = out0 + (unsigned)(((2 * tm + wm) * 192 + tn * 12 + 6 * wn + h) * 1024 + r * 16);
#pragma unroll
        for (int tb = 0; tb < 2; ++tb) {
            const float rs = trs[64 * wm + 32 * tb + r], rs2 = rs * rs;
#pragma unroll
            for (int j = 0; j < 3; ++j) {
                unsigned pk[2][2];
#pragma unroll
                for (int qd = 0; qd < 2; ++qd) {
                    float hv[4];
#pragma unroll
                    for (int i = 0; i < 4; ++i) hv[i] = fmaxf(acc[j][tb][4 * qd + i] * rs2, 0.f) * acc[j][tb][8 + 4 * qd + i];
                    pk[qd][0] = pack2(hv[0], hv[1]); pk[qd][1] = pack2(hv[2], hv[3]);
                }
                auto s0 = __builtin_amdgcn_permlane32_swap(pk[0][0], pk[1][0], false, false);
                auto s1 = __builtin_amdgcn_permlane32_swap(pk[0][1], pk[1][1], false, false);
                st128(hp + j * 2048 + tb * 512, (u32x4){s0[0], s1[0], s0[1], s1[1]});
                SBAR();
            }
        }
    } else if (tn < 10) {
        STAMPW(1, 8); ln_mainloop<MODE, FIRST, true>(smem, acc, wm, wn, lane, ws, tm, tn); STAMPW(2, 8);
        const float sc = tn < 5 ? QSCALE : 1.f;
        char* qp = out0 + (unsigned)(((2 * tm + wm) * 160 + tn * 16 + 8 * wn + h) * 1024 + r * 16);
#pragma unroll
        for (int tb = 0; tb < 2; ++tb) {
            const int tba = FIRST ? tb ^ wn : tb;
            const float rs = trs[64 * wm + 32 * tba + r] * sc;
#pragma unroll
            for (int cb = 0; cb < 2; ++cb)
#pragma unroll
                for (int qp2 = 0; qp2 < 2; ++qp2) {
                    const f32x16& a = acc[cb][tb];
                    auto s0 = __builtin_amdgcn_permlane32_swap(pack2(a[8 * qp2] * rs, a[8 * qp2 + 1] * rs), pack2(a[8 * qp2 + 4] * rs, a[8 * qp2 + 5] * rs), false, false);
                    auto s1 = __builtin_amdgcn_permlane32_swap(pack2(a[8 * qp2 + 2] * rs, a[8 * qp2 + 3] * rs), pack2(a[8 * qp2 + 6] * rs, a[8 * qp2 + 7] * rs), false, false);
                    st128(qp + (4 * (FIRST ? cb ^ wm : cb) + 2 * qp2) * 1024 + tba * 512, (u32x4){s0[0], s1[0], s0[1], s1[1]});
                    SBAR();
                }
        }
    } else {
        STAMPW(1, 8); ln_mainloop<MODE, FIRST, false>(smem, acc, wm, wn, lane, ws, tm, tn); STAMPW(2, 8);
        char* vp = out1 + (unsigned)((((tm * 8 + 4 * wm) * 2 + h) * 640 + (tn - 10) * 128 + 64 * wn + r) * 16);
#pragma unroll
        for (int tb = 0; tb < 2; ++tb)
#pragma unroll
            for (int s = 0; s < 2; ++s) {
                const int tba = FIRST ? tb ^ wn : tb, tl = 64 * wm + 32 * tba + 16 * s + 4 * h;
                const f32x4 r0 = *(LAS const f32x4*)(trs + tl), r1 = *(LAS const f32x4*)(trs + tl + 8);
#pragma unroll
                for (int cb = 0; cb < 2; ++cb) {
                    const f32x16& a = acc[cb][tb];
                    st128(vp + (2 * tba + s) * 20480 + (FIRST ? cb ^ wm : cb) * 512,
                          (u32x4){pack2(a[8 * s] * r0[0], a[8 * s + 1] * r0[1]), pack2(a[8 * s + 2] * r0[2], a[8 * s + 3] * r0[3]),
                                  pack2(a[8 * s + 4] * r1[0], a[8 * s + 5] * r1[1]), pack2(a[8 * s + 6] * r1[2], a[8 * s + 7] * r1[3])});
                    SBAR();
                }
            }
    }
}

template <int KT, bool RESROW, bool OUTROW>
DI void phase_res(LAS char* smem, const char* __restrict__ A, const char* __restrict__ W, const char* __restrict__ res, char* __restrict__ out,
                  char* __restrict__ stats, char* __restrict__ ws DBG_ARG) {
    constexpr int NS = KT / 64, NSLOT = 5, SLOT = 18432, DEP = 3;
    constexpr bool TAILS = KT == DM;
    const int tid = threadIdx.x, lane = tid & 63, w = __builtin_amdgcn_readfirstlane(tid >> 6);
    const int xcd = blockIdx.x & 7, bi = blockIdx.x >> 3, pr = xcd >> 1, e = xcd & 1;
    const int tm = 8 * pr + (bi & 7), tn = 4 * e + (bi >> 3);

    if (w < 8) {
        const int j = w;
        const unsigned off = (unsigned)(j * 1024 + lane * 16);
        const char* ab = A + tm * (KT * 128);
        const char* wb = W + tn * (NS * 10240);
        unsigned sb = (unsigned)(size_t)smem + j * 1024;
        int sl = 0;
        auto issue = [&]() {
            glds16s(ab, off, sb);
            glds16s(wb, off, sb + 8192);
            if (j < 2) glds16s(wb + 8192, off, sb + 16384);
            ab += 8192; wb += 10240;
            if (sl == NSLOT - 1) { sl = 0; sb -= (NSLOT - 1) * SLOT; } else { ++sl; sb += SLOT; }
        };
#define RS_WAIT(n) do { if (j < 2) WAITV(3 * (n)); else WAITV(2 * (n)); } while (0)
        issue(); issue(); issue();
        RS_WAIT(2);
        BAR_RAW();
#pragma unroll 1
        for (int s = 0; s < NS - DEP; ++s) { issue(); RS_WAIT(2); BAR_RAW(); }
        RS_WAIT(1);
        BAR_RAW();
        WAITV(0);
        BAR_RAW();
#undef RS_WAIT
        return;
    }

    const int cw = w - 8, r = lane & 15, g = lane >> 4, row = 16 * cw + r;
    u32x2 rv[5];
#pragma unroll
    for (int j = 0; j < 5; ++j)
        rv[j] = RESROW ? *(const u32x2*)(res + (unsigned)((tm * 64 + row) * 1280 + tn * 160 + j * 32 + g * 8))
                       : *(const u32x2*)(res + (unsigned)((tm * 80 + tn * 10 + 2 * j + (g >> 1)) * 1024 + row * 16 + (g & 1) * 8));
    const unsigned xa = g * 1024 + row * 16, xt = g * 1024 + row * 8, wa = 8192 + lane * 16;
    f32x4 acc[5];
#pragma unroll
    for (int j = 0; j < 5; ++j) acc[j] = (f32x4){0.f, 0.f, 0.f, 0.f};
    bf16x8 xf[2], wf[2][5];
    BAR_RAW();
    STAMPW(1, 8);
#pragma unroll
    for (int ks = 0; ks < 2; ++ks) {
        xf[ks] = lds128(smem + xa + ks * 4096);
#pragma unroll
        for (int j = 0; j < 5; ++j) wf[ks][j] = lds128(smem + wa + ks * 5120 + j * 1024);
    }
    int slot = 0;
#define RS_STAGE(TAIL) do { \
        slot = (slot + 1 == NSLOT) ? 0 : slot + 1; \
        LAS char* nb = smem + slot * SLOT; \
        BAR_RAW();                                   \
        SBAR(); \
        _Pragma("unroll") for (int ks = 0; ks < 2; ++ks) { \
            _Pragma("unroll") for (int j = 0; j < 5; ++j) { \
                acc[j] = MFMA16(wf[ks][j], xf[ks], acc[j]); \
                wf[ks][j] = lds128(nb + wa + ks * 5120 + j * 1024); \
                if (j == 4) xf[ks] = (TAIL) ? cat(lds64(nb + xt + ks * 4096), lds64(nb + xt + ks * 4096 + 512)) : lds128(nb + xa + ks * 4096); \
                SBAR(); \
            } } } while (0)
#pragma unroll 1
    for (int s = 0; s < NS - 1 - (TAILS ? 2 : 0); ++s) RS_STAGE(false);
    if (TAILS) {
#pragma unroll 1
        for (int s = 0; s < 2; ++s) RS_STAGE(true);
    }
#undef RS_STAGE
#pragma unroll
    for (int ks = 0; ks < 2; ++ks)
#pragma unroll
        for (int j = 0; j < 5; ++j) acc[j] = MFMA16(wf[ks][j], xf[ks], acc[j]);
    STAMPW(2, 8);
    u32x2 pk[5];
#pragma unroll
    for (int j = 0; j < 5; ++j)
        pk[j] = (u32x2){pack2(acc[j][0] + bflo(rv[j][0]), acc[j][1] + bfhi(rv[j][0])), pack2(acc[j][2] + bflo(rv[j][1]), acc[j][3] + bfhi(rv[j][1]))};
    if (OUTROW) {
#pragma unroll
        for (int j = 0; j < 5; ++j) st64(out + (unsigned)((tm * 64 + row) * 1280 + tn * 160 + j * 32 + g * 8), pk[j]);
    } else {
        char* op = out + (unsigned)((tm * 80 + tn * 10) * 1024 + row * 16);
#pragma unroll
        for (int jp = 0; jp < 2; ++jp) {
            auto s0 = __builtin_amdgcn_permlane16_swap(pk[2 * jp][0], pk[2 * jp + 1][0], false, false);
            auto s1 = __builtin_amdgcn_permlane16_swap(pk[2 * jp][1], pk[2 * jp + 1][1], false, false);
            st128(op + (4 * jp + 2 * (g & 1) + (g >> 1)) * 1024, (u32x4){s0[0], s1[0], s0[1], s1[1]});
        }
        st64(op + (8 + (g >> 1)) * 1024 + (g & 1) * 8, pk[4]);
        float v[20]; float sum = 0.f;
#pragma unroll
        for (int j = 0; j < 5; ++j) {
            v[4 * j] = bflo(pk[j][0]); v[4 * j + 1] = bfhi(pk[j][0]); v[4 * j + 2] = bflo(pk[j][1]); v[4 * j + 3] = bfhi(pk[j][1]);
            sum += (v[4 * j] + v[4 * j + 1]) + (v[4 * j + 2] + v[4 * j + 3]);
        }
        const float mean = xsum16_32(sum) * (1.f / 80.f);
        float m2 = 0.f;
#pragma unroll
        for (int i = 0; i < 20; ++i) { const float d = v[i] - mean; m2 += d * d; }
        m2 = xsum16_32(m2);
        if (g == 0) st64(stats + (unsigned)((tm * 8 + tn) * 512 + row * 8), (u32x2){__float_as_uint(mean), __float_as_uint(m2)});
    }
}

constexpr int ACH = 23552, A_V = 12288, VSTEP = 704, VHALF = 352, ANSL = 6, A_Q = ANSL * ACH;
DI float swap_max(float m) { auto rr = __builtin_amdgcn_permlane32_swap(__float_as_uint(m), __float_as_uint(m), false, false); return fmaxf(__uint_as_float(rr[0]), __uint_as_float(rr[1])); }
DI float max16(const f32x16& s) {
    float a = fmaxf(fmaxf(s[0], s[1]), s[2]), b = fmaxf(fmaxf(s[3], s[4]), s[5]);
    a = fmaxf(fmaxf(a, s[6]), s[7]); b = fmaxf(fmaxf(b, s[8]), s[9]);
    a = fmaxf(fmaxf(a, s[10]), s[11]); b = fmaxf(fmaxf(b, s[12]), s[13]);
    a = fmaxf(fmaxf(a, s[14]), s[15]);
    return swap_max(fmaxf(a, b));
}
DI bf16x8 packp(const f32x16& p, int s) {
    u32x4 u = {pack2(p[8 * s], p[8 * s + 1]), pack2(p[8 * s + 2], p[8 * s + 3]), pack2(p[8 * s + 4], p[8 * s + 5]), pack2(p[8 * s + 6], p[8 * s + 7])};
    return __builtin_bit_cast(bf16x8, u);
}

DI void retile_slab(int task, LAS char* scr, const Wts& wt, char* ws, int lane) {
    const unsigned sl = (unsigned)(size_t)scr, l16 = lane * 16;
    const int q = (lane & 15) >> 2, pp = lane & 3;
    if (task < 544) {
        const bool iso = task < 160;
        const int t2 = iso ? task : task - 160, nk = iso ? 20 : 48, ct = t2 / nk, ks = t2 - ct * nk;
#pragma unroll
        for (int p = 0; p < 5; ++p) { const int G = p * 64 + lane, kr = G / 10, c = G - kr * 10, k = ks * 32 + kr;
            const int row = !iso ? k : (k < 512 ? 20 * (k >> 4) + (k & 15) : 20 * ((k - 512) >> 2) + 16 + (k & 3));
            glds16s(iso ? wt.wo : wt.wd, (unsigned)(row * 1280 + ct * 160 + c * 16), sl + p * 1024); }
        WAITV(0);
        LAS const char* rp = scr + (8 * (lane >> 4) + q) * 160 + pp * 8;
#pragma unroll
        for (int i = 0; i < 5; ++i) st128s(ws + (iso ? WS_WO : WS_WD) + t2 * 5120 + i * 1024, l16, __builtin_bit_cast(u32x4, cat(ldstr(rp + i * 32), ldstr(rp + 640 + i * 32))));
    } else if (task < 1184) {
        const int t2 = task - 544, tf = t2 / 40, ks = t2 - tf * 40;
#pragma unroll
        for (int p = 0; p < 3; ++p) { const int G = p * 64 + lane, k = G / 12, c = G - k * 12; const unsigned so = (unsigned)((ks * 16 + k) * 3072 + tf * 192 + c * 16);
            glds16s(wt.wg, so, sl + p * 1024); glds16s(wt.wu, so, sl + 3072 + p * 1024); }
        WAITV(0);
        LAS const char* rp = scr + ((lane >> 4) & 1) * 3072 + (8 * (lane >> 5) + q) * 192 + pp * 8;
#pragma unroll
        for (int i = 0; i < 6; ++i) st128s(ws + WS_WGU + t2 * 6144 + i * 1024, l16, __builtin_bit_cast(u32x4, cat(ldstr(rp + i * 32), ldstr(rp + 768 + i * 32))));
    }
}

DI void phase_attn(LAS char* smem, const char* __restrict__ QK, const char* __restrict__ V, char* __restrict__ O, char* __restrict__ ws, const Wts& wt,
                   bool first DBG_ARG) {
    const int tid = threadIdx.x, lane = tid & 63, w = __builtin_amdgcn_readfirstlane(tid >> 6);
    const int xcd = blockIdx.x & 7, bi = blockIdx.x >> 3, pr = xcd >> 1, e = xcd & 1;
    const int head = 16 * e + (bi >> 1), qt = 2 * pr + (bi & 1), par = head & 1;
    const unsigned sm0 = (unsigned)(size_t)smem, l16 = lane * 16;
    const int g0 = 5 * (head >> 1) + 3 * par, gt = 5 * (head >> 1) + 2;
    auto kq_src = [&](int sec, int blk256, int i) { const int grp = i / 3, part = i - 3 * grp; return QK + ((blk256 * 4 + grp) * 160 + sec * 80 + (part == 2 ? gt : g0 + part)) * 1024; };
    auto v_off = [&](int i, bool& ok) { const int G = i * 64 + lane, sh = G / 22, row = G - sh * 22; ok = row < 20; return (unsigned)((sh * 640 + head * 20 + row) * 16); };
    const int c0 = 2 * pr;
    {
        bool ok; const unsigned vo = v_off(w, ok);
        glds16s(kq_src(1, c0, w), l16, sm0 + w * 1024);
        glds16s(kq_src(0, qt, w), l16, sm0 + A_Q + w * 1024);
        if (w < 11 && ok) glds16s(V + c0 * 327680, vo, sm0 + A_V + w * 1024);
        if (tid < ANSL * 64) {
            const unsigned cv = (tid & 1) ? 0u : 0x3F803F80u;
            *(LAS u32x4*)(smem + (tid >> 6) * ACH + A_V + ((tid & 63) >> 1) * VHALF + 320 + (tid & 1) * 16) = (u32x4){cv, cv, cv, cv};
        }
    }
    if (w >= 8) {
        const int lw = w - 8;
        __builtin_amdgcn_s_setprio(3);
        unsigned vo[3]; bool ok[3];
#pragma unroll
        for (int i = 0; i < 3; ++i) vo[i] = v_off(lw + 4 * i, ok[i]);
        auto chunk = [&](int c) {
            const int cc = (c + c0) & 7; const unsigned sb = sm0 + (c >= ANSL ? c - ANSL : c) * ACH;
#pragma unroll
            for (int i = 0; i < 3; ++i) glds16s(kq_src(1, cc, lw + 4 * i), l16, sb + (lw + 4 * i) * 1024);
#pragma unroll
            for (int i = 0; i < 3; ++i) if ((i < 2 || lw < 3) && ok[i]) glds16s(V + cc * 327680, vo[i], sb + A_V + (lw + 4 * i) * 1024);
        };
        asm volatile("s_waitcnt vmcnt(0) lgkmcnt(0)\n\ts_barrier" ::: "memory");
        chunk(1); chunk(2);
        WAITV(0); BAR_RAW();
        chunk(3); chunk(4); chunk(5);
        WAITV(0); BAR_RAW();
        chunk(6); chunk(7);
        if (first) {
            if (lw < 3) retile_slab(blockIdx.x + 256 * lw, smem + A_Q + lw * 6144, wt, ws, lane);
            WAITV(0); BAR_RAW();
            if (lw >= 2) retile_slab(blockIdx.x + 256 * (lw + 1), smem + 2 * ACH + lw * 6144, wt, ws, lane);
        } else {
            WAITV(0);
            const int t = tid - 512;
            unsigned tv = *(const unsigned*)(ws + WS_WO + e * 409600 + bi * 12800 + min(t, 99) * 128);
            tv ^= *(const unsigned*)(ws + WS_WGU + e * 1966080 + bi * 61440 + t * 128);
            tv ^= *(const unsigned*)(ws + WS_WGU + e * 1966080 + bi * 61440 + min(256 + t, 479) * 128);
            BAR_RAW();
            asm volatile("" ::"v"(tv));
        }
        return;
    }

    const int r = lane & 31, h = lane >> 5;
    const unsigned k0a = h * 1024 + r * 16, k1a = 2048 + r * 16 + par * 8, va = A_V + h * VHALF + min(r, 21) * 16;
    f32x16 zero, negm, o;
#pragma unroll
    for (int i = 0; i < 16; ++i) { zero[i] = 0.f; o[i] = 0.f; }
    bf16x8 v1 = {0, 0, 0, 0, 0, 0, 0, 0};
    u32x4 pw0 = {0, 0, 0, 0}, pw1 = {0, 0, 0, 0};
    float e0 = 0.f, e1 = 0.f, e2 = 0.f, e3 = 0.f;
    PIN(o); PIN(v1); PIN(pw0); PIN(pw1);
    asm volatile("s_waitcnt vmcnt(0) lgkmcnt(0)\n\ts_barrier" ::: "memory");
    STAMPW(1, 0);
    bf16x8 q0; bf16x4 q1;
    { LAS const char* qp = smem + A_Q + (w >> 1) * 3072 + (w & 1) * 512;
      q0 = lds128(qp + k0a); q1 = lds64(qp + k1a);
      if (h) q1 = (bf16x4){0, 0, 0, 0}; }
    LAS const char* pk0 = smem + k0a;
    LAS const char* pk1 = smem + k1a;
    LAS const char* pv = smem + va;
    f32x16 sA, sB;
    {
        const bf16x8 a = lds128(pk0); const bf16x4 b = lds64(pk1);
        f32x16 s = MFMA32(a, q0, zero);
        s = MFMA32K8(b, q1, s);
        const float m = max16(s);
#pragma unroll
        for (int i = 0; i < 16; ++i) negm[i] = -m;
        sA = MFMA32(a, q0, negm);
        sA = MFMA32K8(b, q1, sA);
    }
    bf16x8 ka = lds128(pk0 + 512), v0 = lds128(pv);
    bf16x4 kb = lds64(pk1 + 512);
#define EX(x) __builtin_amdgcn_exp2f(x)
#define ATT_STEP(SN, SC, PK0, PK1, PV0, PV1) do { \
        float t0, t1, t2, t3; \
        SN = MFMA32(ka, q0, negm); PIN(SN); ka = lds128(PK0); \
        pw1[2] = pack2(e0, e1); pw1[3] = pack2(e2, e3); PIN(pw1); SBAR(); \
        t0 = EX(SC[0]); t1 = EX(SC[1]); t2 = EX(SC[2]); t3 = EX(SC[3]); PIN(t0); PIN(t1); PIN(t2); PIN(t3); SBAR(); \
        o = MFMA32(v1, __builtin_bit_cast(bf16x8, pw1), o); PIN(o); v1 = lds128(PV1); \
        pw0[0] = pack2(t0, t1); pw0[1] = pack2(t2, t3); PIN(pw0); SBAR(); \
        t0 = EX(SC[4]); t1 = EX(SC[5]); t2 = EX(SC[6]); t3 = EX(SC[7]); PIN(t0); PIN(t1); PIN(t2); PIN(t3); SBAR(); \
        SN = MFMA32K8(kb, q1, SN); PIN(SN); kb = lds64(PK1); \
        pw0[2] = pack2(t0, t1); pw0[3] = pack2(t2, t3); PIN(pw0); SBAR(); \
        t0 = EX(SC[8]); t1 = EX(SC[9]); t2 = EX(SC[10]); t3 = EX(SC[11]); PIN(t0); PIN(t1); PIN(t2); PIN(t3); SBAR(); \
        o = MFMA32(v0, __builtin_bit_cast(bf16x8, pw0), o); PIN(o); v0 = lds128(PV0); \
        pw1[0] = pack2(t0, t1); pw1[1] = pack2(t2, t3); PIN(pw1); SBAR(); \
        e0 = EX(SC[12]); e1 = EX(SC[13]); e2 = EX(SC[14]); e3 = EX(SC[15]); PIN(e0); PIN(e1); PIN(e2); PIN(e3); SBAR(); \
    } while (0)
#define KOF(u) (((u) >> 1) * 3072 + ((u) & 1) * 512)
#pragma unroll 1
    for (int c = 0; c < 8; ++c) {
        const int step = (c == ANSL - 1) ? -(ANSL - 1) * ACH : ACH;
        LAS const char* nk0 = pk0 + step; LAS const char* nk1 = pk1 + step; LAS const char* nv = pv + step;
        if (c == 4) __builtin_amdgcn_s_setprio(2); else if (c == 0 || c == 2 || c == 5 || c == 7) __builtin_amdgcn_s_setprio(1);
        ATT_STEP(sB, sA, pk0 + KOF(2), pk1 + KOF(2), pv + 2 * VSTEP, pv + 1 * VSTEP);
        ATT_STEP(sA, sB, pk0 + KOF(3), pk1 + KOF(3), pv + 4 * VSTEP, pv + 3 * VSTEP);
        ATT_STEP(sB, sA, pk0 + KOF(4), pk1 + KOF(4), pv + 6 * VSTEP, pv + 5 * VSTEP);
        ATT_STEP(sA, sB, pk0 + KOF(5), pk1 + KOF(5), pv + 8 * VSTEP, pv + 7 * VSTEP);
        if (c == 1 || c == 6) __builtin_amdgcn_s_setprio(2); else if (c != 3 && c != 4) __builtin_amdgcn_s_setprio(0);
        ATT_STEP(sB, sA, pk0 + KOF(6), pk1 + KOF(6), pv + 10 * VSTEP, pv + 9 * VSTEP);
        ATT_STEP(sA, sB, pk0 + KOF(7), pk1 + KOF(7), pv + 12 * VSTEP, pv + 11 * VSTEP);
        if (c == 0 || c == 2 || c == 5) { BAR_RAW(); __builtin_amdgcn_s_setprio(3); }
        ATT_STEP(sB, sA, nk0, nk1, pv + 14 * VSTEP, pv + 13 * VSTEP);
        ATT_STEP(sA, sB, nk0 + KOF(1), nk1 + KOF(1), nv, pv + 15 * VSTEP);
        pk0 = nk0; pk1 = nk1; pv = nv;
    }
#undef KOF
#undef ATT_STEP
    pw1[2] = pack2(e0, e1); pw1[3] = pack2(e2, e3);
    o = MFMA32(v1, __builtin_bit_cast(bf16x8, pw1), o);
    STAMPW(2, 0);
    float l;
    { auto rr = __builtin_amdgcn_permlane32_swap(__float_as_uint(o[8]), __float_as_uint(o[8]), false, false); l = __uint_as_float(rr[1]); }
    float chk = 0.f;
#pragma unroll
    for (int i = 0; i < 12; ++i) chk += o[i] * 0.f;
    const bool okr = (chk == 0.f) && (l <= 1.8446744e19f);
    if (__builtin_expect(!__all(okr), 0)) {
        float m = -INFINITY;
#pragma unroll
        for (int i = 0; i < 16; ++i) o[i] = 0.f;
        const unsigned cv = r == 20 ? 0x3F803F80u : 0u;
#pragma unroll 1
        for (int kbk = 0; kbk < 64; ++kbk) {
            const char* kp = QK + ((kbk >> 1) * 160 + 80) * 1024 + ((kbk & 1) * 32 + r) * 16;
            f32x16 s = MFMA32(*(const bf16x8*)(kp + (g0 + h) * 1024), q0, zero);
            s = MFMA32K8(*(const bf16x4*)(kp + gt * 1024 + par * 8), q1, s);
            const float mn = fmaxf(m, max16(s));
            const float al = __builtin_amdgcn_exp2f(m - mn);
            m = mn;
#pragma unroll
            for (int i = 0; i < 16; ++i) { o[i] *= al; s[i] = __builtin_amdgcn_exp2f(s[i] - mn); }
#pragma unroll
            for (int st = 0; st < 2; ++st) {
                u32x4 vv = *(const u32x4*)(V + (((2 * kbk + st) * 2 + h) * 640 + head * 20 + min(r, 19)) * 16);
                if (r >= 20) vv = (u32x4){cv, cv, cv, cv};
                o = MFMA32(__builtin_bit_cast(bf16x8, vv), packp(s, st), o);
            }
        }
        auto rr = __builtin_amdgcn_permlane32_swap(__float_as_uint(o[8]), __float_as_uint(o[8]), false, false); l = __uint_as_float(rr[1]);
    }
    {
        const float inv = 1.f / l;
        char* ob = O + (unsigned)((qt * 4 + (w >> 1)) * 81920);
        const int row = (w & 1) * 32 + r;
        auto s0 = __builtin_amdgcn_permlane32_swap(pack2(o[0] * inv, o[1] * inv), pack2(o[4] * inv, o[5] * inv), false, false);
        auto s1 = __builtin_amdgcn_permlane32_swap(pack2(o[2] * inv, o[3] * inv), pack2(o[6] * inv, o[7] * inv), false, false);
        st128(ob + (unsigned)((2 * head + h) * 1024 + row * 16), (u32x4){s0[0], s1[0], s0[1], s1[1]});
        if (h == 0) st64(ob + (unsigned)(65536 + head * 512 + row * 8), (u32x2){pack2(o[8] * inv, o[9] * inv), pack2(o[10] * inv, o[11] * inv)});
    }
#undef EX
}

#define SMEM_DECL(n) __shared__ __attribute__((aligned(1024))) char smem_[n]; LAS char* smem = (LAS char*)smem_
template <bool FIRST>
__global__ __launch_bounds__(768) void k_qkv(const char* X, const char* wqkv, const char* wo, const char* wg, const char* wu, const char* wd,
                                             char* ws, int layer DBG_ARG) {
    SMEM_DECL(131072);
    STAMPW(0, 8);
    const Wts wt = {wo, wg, wu, wd};
    phase_ln<0, FIRST>(smem, X, wqkv, ws + WS_ST + SZ_ST, ws + WS_QK + layer * 2 * SZ_X, ws + WS_V + layer * SZ_X, ws, wt DBG_PASS(kid));
    STAMPW(3, 8);
}
__global__ __launch_bounds__(768) void k_gu(char* ws, int layer DBG_ARG) {
    SMEM_DECL(LDS_BYTES);
    STAMPW(0, 8);
    const Wts wt = {nullptr, nullptr, nullptr, nullptr};
    phase_ln<1, false>(smem, ws + WS_XA + layer * SZ_X, ws + WS_WGU, ws + WS_ST + 2 * layer * SZ_ST, ws + WS_H + layer * (SEQ * FF * 2), nullptr, ws, wt DBG_PASS(kid));
    STAMPW(3, 8);
}
__global__ __launch_bounds__(768) void k_attn(const char* wo, const char* wg, const char* wu, const char* wd, char* ws, int layer DBG_ARG) {
    SMEM_DECL(LDS_BYTES);
    STAMPW(0, 0);
    const Wts wt = {wo, wg, wu, wd};
    phase_attn(smem, ws + WS_QK + layer * 2 * SZ_X, ws + WS_V + layer * SZ_X, ws + WS_O + layer * SZ_X, ws, wt, layer == 0 DBG_PASS(kid));
    STAMPW(3, 0);
}
template <int KT, bool RESROW, bool OUTROW>
__global__ __launch_bounds__(768) void k_res(const char* A, const char* W, const char* res, char* out, char* stats, char* ws DBG_ARG) {
    SMEM_DECL(5 * 18432);
    STAMPW(0, 8);
    phase_res<KT, RESROW, OUTROW>(smem, A, W, res, out, stats, ws DBG_PASS(kid));
    STAMPW(3, 8);
}

extern "C" void kernel_launch(void* const* d_in, const int* in_sizes, int n_in, void* d_out, int out_size, void* d_ws, size_t ws_size,
                              hipStream_t stream) {
    const char* x0 = (const char*)d_in[0];
    const char* wqkv = (const char*)d_in[1];
    const char* wo = (const char*)d_in[2];
    const char* wg = (const char*)d_in[4];
    const char* wu = (const char*)d_in[5];
    const char* wd = (const char*)d_in[6];
    char* ws = (char*)d_ws;
    char* out = (char*)d_out;
    k_qkv<true><<<256, 768, 0, stream>>>(x0, wqkv, wo, wg, wu, wd, ws, 0 DBG_PASS(1));
    k_attn<<<256, 768, 0, stream>>>(wo, wg, wu, wd, ws, 0 DBG_PASS(2));
    k_res<DM, true, false><<<256, 768, 0, stream>>>(ws + WS_O, ws + WS_WO, x0, ws + WS_XA, ws + WS_ST, ws DBG_PASS(3));
    k_gu<<<256, 768, 0, stream>>>(ws, 0 DBG_PASS(4));
    k_res<FF, false, false><<<256, 768, 0, stream>>>(ws + WS_H, ws + WS_WD, ws + WS_XA, ws + WS_XB, ws + WS_ST + SZ_ST, ws DBG_PASS(5));
    k_qkv<false><<<240, 768, 0, stream>>>(ws + WS_XB, wqkv, wo, wg, wu, wd, ws, 1 DBG_PASS(6));
    k_attn<<<256, 768, 0, stream>>>(wo, wg, wu, wd, ws, 1 DBG_PASS(7));
    k_res<DM, false, false><<<256, 768, 0, stream>>>(ws + WS_O + SZ_X, ws + WS_WO, ws + WS_XB, ws + WS_XA + SZ_X, ws + WS_ST + 2 * SZ_ST, ws DBG_PASS(8));
    k_gu<<<256, 768, 0, stream>>>(ws, 1 DBG_PASS(9));
    k_res<FF, false, true><<<256, 768, 0, stream>>>(ws + WS_H + SEQ * FF * 2, ws + WS_WD, ws + WS_XA + SZ_X, out, ws + WS_ST, ws DBG_PASS(10));
}
```

```cpp
#include <hip/hip_runtime.h>
#include <hip/hip_bf16.h>

typedef short bf16x8 __attribute__((ext_vector_type(8)));
typedef short bf16x4 __attribute__((ext_vector_type(4)));
typedef float f32x16 __attribute__((ext_vector_type(16)));
typedef float f32x4 __attribute__((ext_vector_type(4)));
typedef float f32x2 __attribute__((ext_vector_type(2)));
typedef unsigned u32x4 __attribute__((ext_vector_type(4)));
typedef unsigned u32x2 __attribute__((ext_vector_type(2)));
typedef __bf16 bf2_t __attribute__((ext_vector_type(2)));
#define LAS __attribute__((address_space(3)))
#define GAS __attribute__((address_space(1)))
#define DI __device__ __forceinline__
#define MFMA32(a, b, c) __builtin_amdgcn_mfma_f32_32x32x16_bf16((a), (b), (c), 0, 0, 0)
#define MFMA32K8(a, b, c) __builtin_amdgcn_mfma_f32_32x32x8bf16_1k((a), (b), (c), 0, 0, 0)
#define MFMA4(a, b, c) __builtin_amdgcn_mfma_f32_4x4x4bf16_1k((a), (b), (c), 0, 0, 0)
#define MFMA16(a, b, c) __builtin_amdgcn_mfma_f32_16x16x32_bf16((a), (b), (c), 0, 0, 0)
#define WAITV(n) asm volatile("s_waitcnt vmcnt(%0)" ::"i"(n) : "memory")
#define BAR_LGKM() asm volatile("s_waitcnt lgkmcnt(0)\n\ts_barrier" ::: "memory")
#define BAR_RAW() asm volatile("s_barrier" ::: "memory")
#define PIN(x) asm volatile("" : "+v"(x))
#define SBAR() __builtin_amdgcn_sched_barrier(0)

constexpr int SEQ = 2048, DM = 640, NH = 32, DH = 20, FF = 1536;
constexpr float QSCALE = 0.22360679774997896f * 1.4426950408889634f;
constexpr float LN_EPS = 1e-5f;
constexpr int LDS_BYTES = 163840;

constexpr unsigned SZ_X = SEQ * DM * 2, SZ_ST = 32 * 8 * 512;
constexpr unsigned WS_XA = 0;
constexpr unsigned WS_XB = WS_XA + 2 * SZ_X;
constexpr unsigned WS_O = WS_XB + SZ_X;
constexpr unsigned WS_V = WS_O + 2 * SZ_X;
constexpr unsigned WS_QK = WS_V + 2 * SZ_X;
constexpr unsigned WS_H = WS_QK + 4 * SZ_X;
constexpr unsigned WS_ST = WS_H + 2 * SEQ * FF * 2;
constexpr unsigned WS_WO = WS_ST + 3 * SZ_ST;
constexpr unsigned WS_WD = WS_WO + DM * DM * 2;
constexpr unsigned WS_WGU = WS_WD + FF * DM * 2;
constexpr unsigned WS_CSQ = WS_WGU + 2 * DM * FF * 2;
constexpr unsigned WS_CSGU = WS_CSQ + 8192;
constexpr unsigned WS_CNT = WS_CSGU + 16384;
constexpr int CN_O = 0, CN_X = CN_O + 128, CN_H = CN_X + 384, CN_Q = CN_H + 128, CN_R = CN_Q + 64, CN_END = CN_R + 8;

#define DBG_ARG
#define DBG_PASS(k)
#define STAMPW(i, wv)

#define EXP_ROT
#define NOSTAT 0
#define NOSLAB 0
#define PHASE_IDS() \
    int tid = threadIdx.x, bid = blockIdx.x; asm volatile("" : "+v"(tid), "+s"(bid)); \
    const int lane = tid & 63, w = __builtin_amdgcn_readfirstlane(tid >> 6), xcd = bid & 7, bi = bid >> 3, pr = xcd >> 1, e = xcd & 1
DI unsigned pack2(float a, float b) { f32x2 v = {a, b}; return __builtin_bit_cast(unsigned, __builtin_convertvector(v, bf2_t)); }
DI float bflo(unsigned u) { return __uint_as_float(u << 16); }
DI float bfhi(unsigned u) { return __uint_as_float(u & 0xffff0000u); }
DI unsigned long long sgpr64(const void* p) {
    return ((unsigned long long)(unsigned)__builtin_amdgcn_readfirstlane((int)((unsigned long long)p >> 32)) << 32) |
           (unsigned)__builtin_amdgcn_readfirstlane((int)(unsigned long long)p);
}
template <bool DEV>
DI void glds16s(const char* sb_, unsigned voff, unsigned ldsaddr) {
    const unsigned long long sbase = sgpr64(sb_);
    ldsaddr = __builtin_amdgcn_readfirstlane(ldsaddr);
    if (DEV) asm volatile("s_mov_b32 m0, %2\n\ts_nop 3\n\tglobal_load_lds_dwordx4 %1, %0 sc1" ::"s"(sbase), "v"(voff), "s"(ldsaddr) : "memory");
    else asm volatile("s_mov_b32 m0, %2\n\ts_nop 3\n\tglobal_load_lds_dwordx4 %1, %0" ::"s"(sbase), "v"(voff), "s"(ldsaddr) : "memory");
}
template <bool DEV> DI void st128(char* p, u32x4 v) {
    if (DEV) asm volatile("global_store_dwordx4 %0, %1, off sc1\n\ts_nop 1" ::"v"(p), "v"(v) : "memory"); else *(u32x4*)p = v;
}
template <bool DEV> DI void st64(char* p, u32x2 v) {
    if (DEV) asm volatile("global_store_dwordx2 %0, %1, off sc1" ::"v"(p), "v"(v) : "memory"); else *(u32x2*)p = v;
}
template <bool DEV> DI u32x2 ld64(const char* p) {
    if (DEV) return __builtin_bit_cast(u32x2, __hip_atomic_load((const GAS unsigned long long*)p, __ATOMIC_RELAXED, __HIP_MEMORY_SCOPE_AGENT));
    return *(const u32x2*)p;
}
DI void st128s(char* sb_, unsigned voff, u32x4 v) {
    asm volatile("s_nop 4\n\tglobal_store_dwordx4 %1, %2, %0 sc1\n\ts_nop 1" ::"s"(sgpr64(sb_)), "v"(voff), "v"(v) : "memory");
}
DI void signal(char* ws, int idx, int lane) {
    WAITV(0);
    if (lane == 0) __hip_atomic_fetch_add((GAS unsigned*)(ws + WS_CNT + idx * 128), 1u, __ATOMIC_RELAXED, __HIP_MEMORY_SCOPE_AGENT);
}
DI void await(char* ws, int idx, unsigned target) {
    const GAS unsigned* p = (const GAS unsigned*)(ws + WS_CNT + idx * 128);
    for (unsigned spins = 0; spins < (1u << 20); ++spins) {
        if (__all(__hip_atomic_load(p, __ATOMIC_RELAXED, __HIP_MEMORY_SCOPE_AGENT) >= target)) break;
        __builtin_amdgcn_s_sleep(1);
    }
}
DI bf16x8 lds128(LAS const char* p) { return *(LAS const bf16x8*)p; }
DI bf16x4 lds64(LAS const char* p) { return *(LAS const bf16x4*)p; }
DI bf16x4 ldstr(LAS const char* p) { return __builtin_bit_cast(bf16x4, __builtin_amdgcn_ds_read_tr16_b64_v4i16((LAS bf16x4*)p)); }
DI bf16x8 cat(bf16x4 a, bf16x4 b) { return __builtin_shufflevector(a, b, 0, 1, 2, 3, 4, 5, 6, 7); }
DI float half_sum(float v) { auto a = __builtin_amdgcn_permlane32_swap(__float_as_uint(v), __float_as_uint(v), false, false); return __uint_as_float(a[0]) + __uint_as_float(a[1]); }
DI float xsum16_32(float v) {
    auto a = __builtin_amdgcn_permlane16_swap(__float_as_uint(v), __float_as_uint(v), false, false);
    return half_sum(__uint_as_float(a[0]) + __uint_as_float(a[1]));
}
DI u32x4 split3(float v, bool rowside) {
    const unsigned p1 = pack2(v, v); const float r1 = v - bflo(p1);
    const unsigned p2 = pack2(r1, r1); const float r2 = r1 - bflo(p2);
    const unsigned p3 = pack2(r2, r2);
    const unsigned l1 = p1 & 0xffffu, l2 = p2 & 0xffffu, h1 = p1 & 0xffff0000u, h2 = p2 & 0xffff0000u, h3 = p3 & 0xffff0000u;
    return rowside ? (u32x4){p1, l2 | h1, l2 | h3, 0u} : (u32x4){l1 | h2, l1 | h3, l2 | h1, 0u};
}
DI bf16x4 lo4(bf16x8 v) { return __builtin_shufflevector(v, v, 0, 1, 2, 3); }
DI bf16x4 hi4(bf16x8 v) { return __builtin_shufflevector(v, v, 4, 5, 6, 7); }

template <int MODE> struct LnCfg;
template <> struct LnCfg<0> { static constexpr int SLOT = 32768, NB = 2, PPW = 4, NCS = 128; };
template <> struct LnCfg<1> { static constexpr int SLOT = 40960, NB = 3, PPW = 5, NCS = 192; };
constexpr int T_XC = 0, T_WC = 2048, T_RS = 5120, T_RAW = 5632;

template <int MODE, bool FIRST, bool SWAP>
DI void ln_mainloop(LAS char* smem, f32x16 (&acc)[LnCfg<MODE>::NB][2], int wm, int wn, int lane, char* ws, int tm, int tn) {
    using C = LnCfg<MODE>;
    constexpr int NB = C::NB;
    const int r = lane & 31, h = lane >> 5, q = (lane & 15) >> 2, pp = lane & 3, blk = (lane >> 4) & 1;
    unsigned xa[4], wa[2];
    if (FIRST) {
        const int row = 64 * wm + 32 * wn + r, sw = (row >> 1) & 7;
#pragma unroll
        for (int ks = 0; ks < 4; ++ks) xa[ks] = row * 128 + (((2 * ks + h) ^ sw) << 4);
    } else xa[0] = wm * 8192 + h * 1024 + r * 16;
    if (MODE == 0) {
#pragma unroll
        for (int cb = 0; cb < 2; ++cb) wa[cb] = 16384 + (8 * h + q) * 256 + (((2 * wn + (FIRST ? cb ^ wm : cb)) ^ q) * 64) + blk * 32 + pp * 8;
    } else { wa[0] = 16384 + wn * 3072 + lane * 16; wa[1] = 0; }
    bf16x8 xf[2][2], wf[2][NB];
    f32x4 sx = {0.f, 0.f, 0.f, 0.f}, sw_ = sx; f32x16 sxx;
#pragma unroll
    for (int i = 0; i < 16; ++i) sxx[i] = 0.f;
    const short sv = ((lane & 15) == ((lane >> 4) & 1)) ? (short)0x3F80 : (short)0;
    const bf16x8 sel = {sv, sv, sv, sv, sv, sv, sv, sv};
#define LN_READ(BUF, SB, KS) do { \
        _Pragma("unroll") for (int tb = 0; tb < 2; ++tb) xf[BUF][tb] = FIRST ? lds128((SB) + (xa[KS] ^ (tb * 4096))) : lds128((SB) + xa[0] + (KS) * 2048 + tb * 512); \
        _Pragma("unroll") for (int j = 0; j < NB; ++j) { \
            if (MODE == 0) wf[BUF][j] = cat(ldstr((SB) + wa[j] + (KS) * 4096), ldstr((SB) + wa[j] + (KS) * 4096 + 1024)); \
            else wf[BUF][j] = lds128((SB) + wa[0] + (KS) * 6144 + j * 1024); } \
    } while (0)
#define LN_MMA(BUF) do { \
        _Pragma("unroll") for (int j = 0; j < NB; ++j) _Pragma("unroll") for (int tb = 0; tb < 2; ++tb) \
            acc[j][tb] = SWAP ? MFMA32(wf[BUF][j], xf[BUF][tb], acc[j][tb]) : MFMA32(xf[BUF][tb], wf[BUF][j], acc[j][tb]); \
        if (FIRST && !NOSTAT) { sx = MFMA16(sel, xf[BUF][0], sx); sw_ = MFMA16(sel, wf[BUF][0], sw_); sxx = MFMA32(xf[BUF][0], xf[BUF][0], sxx); } \
    } while (0)
#define LN_STEP(RB, SB, KS, MB) do { LN_READ(RB, SB, KS); LN_MMA(MB); \
        _Pragma("unroll") for (int i_ = 0; i_ < 3; ++i_) { __builtin_amdgcn_sched_group_barrier(0x8, 1, 0); __builtin_amdgcn_sched_group_barrier(0x100, 2, 0); } \
        __builtin_amdgcn_sched_group_barrier(0x8, 2 * NB - 3 + (FIRST ? 3 : 0), 0); \
        SBAR(); } while (0)
    BAR_RAW();
    LN_READ(0, smem, 0);
#pragma unroll 1
    for (int s = 0; s < 9; ++s) {
        LAS char* sb = smem + (s & 3) * C::SLOT;
        LAS char* nb = smem + ((s + 1) & 3) * C::SLOT;
        SBAR();
        LN_STEP(1, sb, 1, 0);
        LN_STEP(0, sb, 2, 1);
        LN_STEP(1, sb, 3, 0);
        BAR_RAW();
        SBAR();
        LN_STEP(0, nb, 0, 1);
    }
    LAS char* tab = smem + 3 * C::SLOT;
    {
        LAS char* sb = smem + C::SLOT;
        SBAR();
        LN_STEP(1, sb, 1, 0);
        LN_STEP(0, sb, 2, 1);
        LN_STEP(1, sb, 3, 0);
        if (!FIRST) BAR_LGKM();
        LN_MMA(1);
    }
    if (FIRST) {
        LAS float* raw = (LAS float*)(tab + T_RAW) + (wn * 2 + wm) * 96;
        if (lane < 16) { raw[lane] = sx[0]; raw[16 + lane] = sx[1]; raw[32 + lane] = sw_[0]; raw[48 + lane] = sw_[1]; }
        { const int ri = (r & 3) + 4 * (r >> 3); float dg = sxx[0];
#pragma unroll
          for (int i = 1; i < 16; ++i) dg = ri == i ? sxx[i] : dg;
          if (((r >> 2) & 1) == h) raw[64 + r] = dg; }
        asm volatile("s_waitcnt lgkmcnt(0)" ::: "memory");
        const float csum = raw[32 + r];
        const float mu = raw[r] * (1.f / DM), var = fmaxf(raw[64 + r] * (1.f / DM) - mu * mu, 0.f);
        const int tl = 64 * wm + 32 * wn + r, nl = 64 * wn + 32 * wm + r;
        if (h == 0) { *(LAS u32x4*)(tab + T_XC + tl * 16) = split3(-mu, true); *(LAS float*)(tab + T_RS + tl * 4) = __builtin_amdgcn_rsqf(var + LN_EPS); }
        else { *(LAS u32x4*)(tab + T_WC + nl * 16) = split3(csum, false); if (tm == 0) *(float*)(ws + WS_CSQ + (tn * 128 + nl) * 4) = csum; }
        BAR_LGKM();
    }
    {
        bf16x4 xc[2], wc[NB];
#pragma unroll
        for (int tb = 0; tb < 2; ++tb) xc[tb] = lds64(tab + T_XC + (64 * wm + 32 * (FIRST ? tb ^ wn : tb) + r) * 16 + h * 8);
#pragma unroll
        for (int j = 0; j < NB; ++j) wc[j] = lds64(tab + T_WC + ((NB * wn + (FIRST ? j ^ wm : j)) * 32 + r) * 16 + h * 8);
#pragma unroll
        for (int j = 0; j < NB; ++j)
#pragma unroll
            for (int tb = 0; tb < 2; ++tb) acc[j][tb] = SWAP ? MFMA32K8(wc[j], xc[tb], acc[j][tb]) : MFMA32K8(xc[tb], wc[j], acc[j][tb]);
    }
#undef LN_STEP
#undef LN_READ
#undef LN_MMA
}

struct Wts { const char *wo, *wg, *wu, *wd; };

template <int MODE, bool FIRST, bool MEGA>
DI void phase_ln(LAS char* smem, const char* X, const char* W0, const char* stats, char* out0, char* out1, char* ws, const Wts& wt,
                 int cin, int cout DBG_ARG) {
    using C = LnCfg<MODE>;
    PHASE_IDS();
    const unsigned sm0 = (unsigned)(size_t)smem;
    int tm, tn;
    if (MODE == 0) { const int lin = e * 30 + bi; tm = 4 * pr + (lin & 3); tn = lin >> 2; }
    else { tm = 4 * pr + (bi & 3); tn = 8 * e + (bi >> 2); }
    LAS char* tab = smem + 3 * C::SLOT;

    if (MODE == 0 && bi >= 30) {
        if (FIRST) {
            const int blk = (xcd * 2 + bi - 30) * 3 + (tid >> 8), m = blk / 24, c = tid & 7, rg = (tid & 255) >> 3;
            const char* p = (m ? wt.wu : wt.wg) + rg * 3072 + (blk - 24 * m) * 128 + c * 16;
            float s[8];
#pragma unroll
            for (int i = 0; i < 8; ++i) s[i] = 0.f;
            u32x4 v[20];
#pragma unroll
            for (int j = 0; j < 20; ++j) v[j] = *(const u32x4*)(p + j * 32 * 3072);
#pragma unroll
            for (int j = 0; j < 20; ++j)
#pragma unroll
                for (int i = 0; i < 4; ++i) { s[2 * i] += bflo(v[j][i]); s[2 * i + 1] += bfhi(v[j][i]); }
            LAS float* red = (LAS float*)smem;
#pragma unroll
            for (int i = 0; i < 8; ++i) red[rg * 192 + (tid >> 8) * 64 + c * 8 + i] = s[i];
            BAR_LGKM();
            if (tid < 192) {
                float t = 0.f;
#pragma unroll
                for (int i = 0; i < 32; ++i) t += red[i * 192 + tid];
                const int b2 = (xcd * 2 + bi - 30) * 3 + (tid >> 6), m2 = b2 / 24, f = (b2 - 24 * m2) * 64 + (tid & 63), tf = f / 96, fl = f - tf * 96;
                *(float*)(ws + WS_CSGU + (tf * 192 + (fl >> 4) * 32 + m2 * 16 + (fl & 15)) * 4) = t;
            }
        }
        return;
    }

    if (w < 8) {
        const int j = w;
        unsigned off[C::PPW];
#pragma unroll
        for (int p = 0; p < 2; ++p) {
            if (FIRST) { const int row = (j + 8 * p) * 8 + (lane >> 3); off[p] = (unsigned)((tm * 128 + row) * 1280 + (((lane & 7) ^ ((row >> 1) & 7)) << 4)); }
            else off[p] = (unsigned)((2 * tm + p) * 81920 + j * 1024 + lane * 16);
        }
#pragma unroll
        for (int p = 2; p < C::PPW; ++p) {
            const int piece = j + 8 * (p - 2);
            if (MODE == 0) { const int k = piece * 4 + (lane >> 4), g = lane & 15;
                off[p] = (unsigned)(k * 3840 + tn * 256 + (((g >> 2) ^ (k & 3)) << 6) + (g & 3) * 16); }
            else off[p] = (unsigned)(tn * 245760 + piece * 1024 + lane * 16);
        }
        constexpr int XSTAGE = FIRST ? 128 : 8192, WSTAGE = MODE == 0 ? 64 * 3840 : 24576;
        int kb = FIRST ? (3 * (tm & 3) + 7 * tn) % 10 : 0;
        constexpr bool WDEV = MEGA && MODE == 1;
        auto issue = [&](int s) {
            const unsigned sb = sm0 + (s & 3) * C::SLOT + j * 1024;
#pragma unroll
            for (int p = 0; p < 2; ++p) glds16s<MEGA>(X + kb * XSTAGE, off[p], sb + p * 8192);
#pragma unroll
            for (int p = 2; p < C::PPW; ++p) glds16s<WDEV>(W0 + kb * WSTAGE, off[p], sb + p * 8192);
            kb = (kb == 9) ? 0 : kb + 1;
        };
        if (FIRST) {
            issue(0); issue(1);
            WAITV(C::PPW);
            BAR_RAW();
#pragma unroll 1
            for (int s = 0; s < 8; ++s) { issue(s + 2); WAITV(C::PPW); BAR_RAW(); }
            WAITV(0);
            BAR_RAW();
            BAR_RAW();
        } else {
            if (MEGA) {
#pragma unroll
                for (int s = 0; s < 4; ++s)
#pragma unroll
                    for (int p = 2; p < C::PPW; ++p) glds16s<WDEV>(W0 + s * WSTAGE, off[p], sm0 + s * C::SLOT + j * 1024 + p * 8192);
                BAR_RAW();
#pragma unroll
                for (int s = 0; s < 4; ++s)
#pragma unroll
                    for (int p = 0; p < 2; ++p) glds16s<true>(X + s * XSTAGE, off[p], sm0 + s * C::SLOT + j * 1024 + p * 8192);
                kb = 4;
            } else { issue(0); issue(1); }
            asm volatile("" ::: "memory");
            const char* sp = stats + (2 * tm + (tid >> 8)) * 4096 + (tid & 3) * 1024 + ((tid >> 2) & 63) * 8;
            const f32x2 st0 = __builtin_bit_cast(f32x2, ld64<MEGA>(sp)), st1 = __builtin_bit_cast(f32x2, ld64<MEGA>(sp + 512));
            const float cs = *(const float*)(ws + (MODE == 0 ? WS_CSQ : WS_CSGU) + (tn * C::NCS + (tid < C::NCS ? tid : 0)) * 4);
            if (MEGA) { WAITV(9); BAR_RAW(); WAITV(7); BAR_RAW(); WAITV(5); BAR_RAW(); }
            else { WAITV(C::PPW + 3); BAR_RAW(); }
#pragma unroll 1
            for (int s = MEGA ? 2 : 0; s < 8; ++s) { issue(s + 2); WAITV(C::PPW); BAR_RAW(); }
            WAITV(0);
            BAR_RAW();
            float sm = st0[0] + st1[0];
            sm += __shfl_xor(sm, 1); sm += __shfl_xor(sm, 2);
            const float mu = sm * 0.125f, d0 = st0[0] - mu, d1 = st1[0] - mu;
            float m2 = st0[1] + st1[1] + 80.f * (d0 * d0 + d1 * d1);
            m2 += __shfl_xor(m2, 1); m2 += __shfl_xor(m2, 2);
            if ((tid & 3) == 0) { *(LAS u32x4*)(tab + T_XC + (tid >> 2) * 16) = split3(-mu, true);
                                  *(LAS float*)(tab + T_RS + (tid >> 2) * 4) = __builtin_amdgcn_rsqf(m2 * (1.f / DM) + LN_EPS); }
            if (tid < C::NCS) *(LAS u32x4*)(tab + T_WC + tid * 16) = split3(cs, false);
            BAR_LGKM();
        }
        return;
    }

    const int wm = w & 1, wn = (w >> 1) & 1, r = lane & 31, h = lane >> 5;
    LAS const float* trs = (LAS const float*)(tab + T_RS);
    if (MEGA) {
        if (w == 8) await(ws, cin + 8 * tm + (lane & 7), 8);
        BAR_RAW();
    }
    f32x16 acc[C::NB][2];
#pragma unroll
    for (int j = 0; j < C::NB; ++j)
#pragma unroll
        for (int tb = 0; tb < 2; ++tb)
#pragma unroll
            for (int i = 0; i < 16; ++i) acc[j][tb][i] = 0.f;

    if (MODE == 1) {
        STAMPW(1, 8); ln_mainloop<MODE, FIRST, true>(smem, acc, wm, wn, lane, ws, tm, tn); STAMPW(2, 8);
        char* hp = out0 + (unsigned)(((2 * tm + wm) * 192 + tn * 12 + 6 * wn + h) * 1024 + r * 16);
#pragma unroll
        for (int tb = 0; tb < 2; ++tb) {
            const float rs = trs[64 * wm + 32 * tb + r], rs2 = rs * rs;
#pragma unroll
            for (int j = 0; j < 3; ++j) {
                unsigned pk[2][2];
#pragma unroll
                for (int qd = 0; qd < 2; ++qd) {
                    float hv[4];
#pragma unroll
                    for (int i = 0; i < 4; ++i) hv[i] = fmaxf(acc[j][tb][4 * qd + i] * rs2, 0.f) * acc[j][tb][8 + 4 * qd + i];
                    pk[qd][0] = pack2(hv[0], hv[1]); pk[qd][1] = pack2(hv[2], hv[3]);
                }
                auto s0 = __builtin_amdgcn_permlane32_swap(pk[0][0], pk[1][0], false, false);
                auto s1 = __builtin_amdgcn_permlane32_swap(pk[0][1], pk[1][1], false, false);
                st128<MEGA>(hp + j * 2048 + tb * 512, (u32x4){s0[0], s1[0], s0[1], s1[1]});
                SBAR();
            }
        }
    } else if (tn < 10) {
        STAMPW(1, 8); ln_mainloop<MODE, FIRST, true>(smem, acc, wm, wn, lane, ws, tm, tn); STAMPW(2, 8);
        const float sc = tn < 5 ? QSCALE : 1.f;
        char* qp = out0 + (unsigned)(((2 * tm + wm) * 160 + tn * 16 + 8 * wn + h) * 1024 + r * 16);
#pragma unroll
        for (int tb = 0; tb < 2; ++tb) {
            const int tba = FIRST ? tb ^ wn : tb;
            const float rs = trs[64 * wm + 32 * tba + r] * sc;
#pragma unroll
            for (int cb = 0; cb < 2; ++cb)
#pragma unroll
                for (int qp2 = 0; qp2 < 2; ++qp2) {
                    const f32x16& a = acc[cb][tb];
                    auto s0 = __builtin_amdgcn_permlane32_swap(pack2(a[8 * qp2] * rs, a[8 * qp2 + 1] * rs), pack2(a[8 * qp2 + 4] * rs, a[8 * qp2 + 5] * rs), false, false);
                    auto s1 = __builtin_amdgcn_permlane32_swap(pack2(a[8 * qp2 + 2] * rs, a[8 * qp2 + 3] * rs), pack2(a[8 * qp2 + 6] * rs, a[8 * qp2 + 7] * rs), false, false);
                    st128<MEGA>(qp + (4 * (FIRST ? cb ^ wm : cb) + 2 * qp2) * 1024 + tba * 512, (u32x4){s0[0], s1[0], s0[1], s1[1]});
                    SBAR();
                }
        }
    } else {
        STAMPW(1, 8); ln_mainloop<MODE, FIRST, false>(smem, acc, wm, wn, lane, ws, tm, tn); STAMPW(2, 8);
        char* vp = out1 + (unsigned)((((tm * 8 + 4 * wm) * 2 + h) * 640 + (tn - 10) * 128 + 64 * wn + r) * 16);
#pragma unroll
        for (int tb = 0; tb < 2; ++tb)
#pragma unroll
            for (int s = 0; s < 2; ++s) {
                const int tba = FIRST ? tb ^ wn : tb, tl = 64 * wm + 32 * tba + 16 * s + 4 * h;
                const f32x4 r0 = *(LAS const f32x4*)(trs + tl), r1 = *(LAS const f32x4*)(trs + tl + 8);
#pragma unroll
                for (int cb = 0; cb < 2; ++cb) {
                    const f32x16& a = acc[cb][tb];
                    st128<MEGA>(vp + (2 * tba + s) * 20480 + (FIRST ? cb ^ wm : cb) * 512,
                          (u32x4){pack2(a[8 * s] * r0[0], a[8 * s + 1] * r0[1]), pack2(a[8 * s + 2] * r0[2], a[8 * s + 3] * r0[3]),
                                  pack2(a[8 * s + 4] * r1[0], a[8 * s + 5] * r1[1]), pack2(a[8 * s + 6] * r1[2], a[8 * s + 7] * r1[3])});
                    SBAR();
                }
            }
    }
    if (MEGA) signal(ws, cout + (MODE == 1 ? (2 * tm + wm) * 2 + wn : tn * 4 + (w - 8)), lane);
}

template <int KT, bool MEGA>
DI void phase_res(LAS char* smem, const char* A, const char* W, const char* res, char* out, char* stats, char* ws, bool resrow, bool outrow,
                  u32x2 (&rv)[5], int cin, int cout, bool wready DBG_ARG) {
    constexpr int NS = KT / 64, NSLOT = 5, SLOT = 18432, DEP = 3;
    constexpr bool TAILS = KT == DM;
    PHASE_IDS();
    const int tm = 8 * pr + (bi & 7), tn = 4 * e + (bi >> 3);

    if (w < 8) {
        const int j = w;
        const unsigned off = (unsigned)(j * 1024 + lane * 16);
        const char* ab = A + tm * (KT * 128);
        const char* wb = W + tn * (NS * 10240);
        unsigned sb = (unsigned)(size_t)smem + j * 1024;
        int sl = 0;
        auto issue = [&]() {
            glds16s<MEGA>(ab, off, sb);
            glds16s<MEGA>(wb, off, sb + 8192);
            if (j < 2) glds16s<MEGA>(wb + 8192, off, sb + 16384);
            ab += 8192; wb += 10240;
            if (sl == NSLOT - 1) { sl = 0; sb -= (NSLOT - 1) * SLOT; } else { ++sl; sb += SLOT; }
        };
#define RS_WAIT(n, x) do { if (j < 2) WAITV(3 * (n) + (x)); else WAITV(2 * (n) + (x)); } while (0)
        if (MEGA) {
            if (!wready) BAR_RAW();
#pragma unroll
            for (int s = 0; s < DEP; ++s) {
                glds16s<true>(wb + s * 10240, off, sb + s * SLOT + 8192);
                if (j < 2) glds16s<true>(wb + s * 10240 + 8192, off, sb + s * SLOT + 16384);
            }
            if (wready) BAR_RAW();
#pragma unroll
            for (int s = 0; s < DEP; ++s) glds16s<true>(ab + s * 8192, off, sb + s * SLOT);
            ab += DEP * 8192; wb += DEP * 10240; sb += DEP * SLOT; sl = DEP;
            WAITV(2);
            BAR_RAW();
            issue(); RS_WAIT(1, 1); BAR_RAW();
        } else {
            issue(); issue(); issue();
            RS_WAIT(2, 0);
            BAR_RAW();
        }
#pragma unroll 1
        for (int s = MEGA ? 1 : 0; s < NS - DEP; ++s) { issue(); RS_WAIT(2, 0); BAR_RAW(); }
        RS_WAIT(1, 0);
        BAR_RAW();
        WAITV(0);
        BAR_RAW();
        if (MEGA) BAR_RAW();
#undef RS_WAIT
        return;
    }

    const int cw = w - 8, r = lane & 15, g = lane >> 4, row = 16 * cw + r;
    if (resrow) {
#pragma unroll
        for (int j = 0; j < 5; ++j) rv[j] = *(const u32x2*)(res + (unsigned)((tm * 64 + row) * 1280 + tn * 160 + j * 32 + g * 8));
    } else if (!MEGA) {
#pragma unroll
        for (int j = 0; j < 5; ++j) rv[j] = *(const u32x2*)(res + (unsigned)((tm * 80 + tn * 10 + 2 * j + (g >> 1)) * 1024 + row * 16 + (g & 1) * 8));
    }
    if (MEGA) {
        if (w == 8) {
            const bool rt = !wready && lane >= 2 && lane < 10;
            await(ws, rt ? CN_R + lane - 2 : cin + 2 * tm + (lane & 1), rt ? 128 : (TAILS ? 32 : 16));
        }
        BAR_RAW();
    }
    const unsigned xa = g * 1024 + row * 16, xt = g * 1024 + row * 8, wa = 8192 + lane * 16;
    f32x4 acc[5];
#pragma unroll
    for (int j = 0; j < 5; ++j) acc[j] = (f32x4){0.f, 0.f, 0.f, 0.f};
    bf16x8 xf[2], wf[2][5];
    BAR_RAW();
    STAMPW(1, 8);
#pragma unroll
    for (int ks = 0; ks < 2; ++ks) {
        xf[ks] = lds128(smem + xa + ks * 4096);
#pragma unroll
        for (int j = 0; j < 5; ++j) wf[ks][j] = lds128(smem + wa + ks * 5120 + j * 1024);
    }
    int slot = 0;
#define RS_STAGE(TAIL) do { \
        slot = (slot + 1 == NSLOT) ? 0 : slot + 1; \
        LAS char* nb = smem + slot * SLOT; \
        BAR_RAW();                                   \
        SBAR(); \
        _Pragma("unroll") for (int ks = 0; ks < 2; ++ks) { \
            _Pragma("unroll") for (int j = 0; j < 5; ++j) { \
                acc[j] = MFMA16(wf[ks][j], xf[ks], acc[j]); \
                wf[ks][j] = lds128(nb + wa + ks * 5120 + j * 1024); \
                if (j == 4) xf[ks] = (TAIL) ? cat(lds64(nb + xt + ks * 4096), lds64(nb + xt + ks * 4096 + 512)) : lds128(nb + xa + ks * 4096); \
                SBAR(); \
            } } } while (0)
#pragma unroll 1
    for (int s = 0; s < NS - 1 - (TAILS ? 2 : 0); ++s) RS_STAGE(false);
    if (TAILS) {
#pragma unroll 1
        for (int s = 0; s < 2; ++s) RS_STAGE(true);
    }
#undef RS_STAGE
    if (MEGA) BAR_LGKM();
#pragma unroll
    for (int ks = 0; ks < 2; ++ks)
#pragma unroll
        for (int j = 0; j < 5; ++j) acc[j] = MFMA16(wf[ks][j], xf[ks], acc[j]);
    STAMPW(2, 8);
#pragma unroll
    for (int j = 0; j < 5; ++j)
        rv[j] = (u32x2){pack2(acc[j][0] + bflo(rv[j][0]), acc[j][1] + bfhi(rv[j][0])), pack2(acc[j][2] + bflo(rv[j][1]), acc[j][3] + bfhi(rv[j][1]))};
    if (outrow) {
#pragma unroll
        for (int j = 0; j < 5; ++j) st64<false>(out + (unsigned)((tm * 64 + row) * 1280 + tn * 160 + j * 32 + g * 8), rv[j]);
    } else {
        char* op = out + (unsigned)((tm * 80 + tn * 10) * 1024 + row * 16);
#pragma unroll
        for (int jp = 0; jp < 2; ++jp) {
            auto s0 = __builtin_amdgcn_permlane16_swap(rv[2 * jp][0], rv[2 * jp + 1][0], false, false);
            auto s1 = __builtin_amdgcn_permlane16_swap(rv[2 * jp][1], rv[2 * jp + 1][1], false, false);
            st128<MEGA>(op + (4 * jp + 2 * (g & 1) + (g >> 1)) * 1024, (u32x4){s0[0], s1[0], s0[1], s1[1]});
        }
        st64<MEGA>(op + (8 + (g >> 1)) * 1024 + (g & 1) * 8, rv[4]);
        float v[20]; float sum = 0.f;
#pragma unroll
        for (int j = 0; j < 5; ++j) {
            v[4 * j] = bflo(rv[j][0]); v[4 * j + 1] = bfhi(rv[j][0]); v[4 * j + 2] = bflo(rv[j][1]); v[4 * j + 3] = bfhi(rv[j][1]);
            sum += (v[4 * j] + v[4 * j + 1]) + (v[4 * j + 2] + v[4 * j + 3]);
        }
        const float mean = xsum16_32(sum) * (1.f / 80.f);
        float m2 = 0.f;
#pragma unroll
        for (int i = 0; i < 20; ++i) { const float d = v[i] - mean; m2 += d * d; }
        m2 = xsum16_32(m2);
        if (g == 0) st64<MEGA>(stats + (unsigned)((tm * 8 + tn) * 512 + row * 8), (u32x2){__float_as_uint(mean), __float_as_uint(m2)});
        if (MEGA) signal(ws, cout + 4 * tm + cw, lane);
    }
}

constexpr int ACH = 23552, A_V = 12288, VSTEP = 704, VHALF = 352, ANSL = 6, A_Q = ANSL * ACH;
DI float swap_max(float m) { auto rr = __builtin_amdgcn_permlane32_swap(__float_as_uint(m), __float_as_uint(m), false, false); return fmaxf(__uint_as_float(rr[0]), __uint_as_float(rr[1])); }
DI float max16(const f32x16& s) {
    float a = fmaxf(fmaxf(s[0], s[1]), s[2]), b = fmaxf(fmaxf(s[3], s[4]), s[5]);
    a = fmaxf(fmaxf(a, s[6]), s[7]); b = fmaxf(fmaxf(b, s[8]), s[9]);
    a = fmaxf(fmaxf(a, s[10]), s[11]); b = fmaxf(fmaxf(b, s[12]), s[13]);
    a = fmaxf(fmaxf(a, s[14]), s[15]);
    return swap_max(fmaxf(a, b));
}
DI bf16x8 packp(const f32x16& p, int s) {
    u32x4 u = {pack2(p[8 * s], p[8 * s + 1]), pack2(p[8 * s + 2], p[8 * s + 3]), pack2(p[8 * s + 4], p[8 * s + 5]), pack2(p[8 * s + 6], p[8 * s + 7])};
    return __builtin_bit_cast(bf16x8, u);
}

DI void retile_slab(int task, LAS char* scr, const Wts& wt, char* ws, int lane) {
    const unsigned sl = (unsigned)(size_t)scr, l16 = lane * 16;
    const int q = (lane & 15) >> 2, pp = lane & 3;
    if (task < 544) {
        const bool iso = task < 160;
        const int t2 = iso ? task : task - 160, nk = iso ? 20 : 48, ct = t2 / nk, ks = t2 - ct * nk;
#pragma unroll
        for (int p = 0; p < 5; ++p) { const int G = p * 64 + lane, kr = G / 10, c = G - kr * 10, k = ks * 32 + kr;
            const int row = !iso ? k : (k < 512 ? 20 * (k >> 4) + (k & 15) : 20 * ((k - 512) >> 2) + 16 + (k & 3));
            glds16s<false>(iso ? wt.wo : wt.wd, (unsigned)(row * 1280 + ct * 160 + c * 16), sl + p * 1024); }
        WAITV(0);
        LAS const char* rp = scr + (8 * (lane >> 4) + q) * 160 + pp * 8;
#pragma unroll
        for (int i = 0; i < 5; ++i) st128s(ws + (iso ? WS_WO : WS_WD) + t2 * 5120 + i * 1024, l16, __builtin_bit_cast(u32x4, cat(ldstr(rp + i * 32), ldstr(rp + 640 + i * 32))));
    } else if (task < 1184) {
        const int t2 = task - 544, tf = t2 / 40, ks = t2 - tf * 40;
#pragma unroll
        for (int p = 0; p < 3; ++p) { const int G = p * 64 + lane, k = G / 12, c = G - k * 12; const unsigned so = (unsigned)((ks * 16 + k) * 3072 + tf * 192 + c * 16);
            glds16s<false>(wt.wg, so, sl + p * 1024); glds16s<false>(wt.wu, so, sl + 3072 + p * 1024); }
        WAITV(0);
        LAS const char* rp = scr + ((lane >> 4) & 1) * 3072 + (8 * (lane >> 5) + q) * 192 + pp * 8;
#pragma unroll
        for (int i = 0; i < 6; ++i) st128s(ws + WS_WGU + t2 * 6144 + i * 1024, l16, __builtin_bit_cast(u32x4, cat(ldstr(rp + i * 32), ldstr(rp + 768 + i * 32))));
    }
}

template <bool MEGA>
DI void phase_attn(LAS char* smem, const char* QK, const char* V, char* O, char* ws, const Wts& wt, bool first, int cout DBG_ARG) {
    PHASE_IDS();
    const int head = 16 * e + (bi >> 1), qt = 2 * pr + (bi & 1), par = head & 1;
    const unsigned sm0 = (unsigned)(size_t)smem, l16 = lane * 16;
    const int g0 = 5 * (head >> 1) + 3 * par, gt = 5 * (head >> 1) + 2;
    auto kq_src = [&](int sec, int blk256, int i) { const int grp = i / 3, part = i - 3 * grp; return QK + ((blk256 * 4 + grp) * 160 + sec * 80 + (part == 2 ? gt : g0 + part)) * 1024; };
    auto v_off = [&](int i, bool& ok) { const int G = i * 64 + lane, sh = G / 22, row = G - sh * 22; ok = row < 20; return (unsigned)((sh * 640 + head * 20 + row) * 16); };
    const int c0 = 2 * pr;
    if (MEGA && !first) {
        if (w == 8) {
            const int sec = min(lane >> 3, 2), col = head * 20 + ((lane >> 2) & 1) * 19;
            await(ws, CN_Q + (sec * 5 + (col >> 7)) * 4 + (lane & 3), 16);
        }
        BAR_RAW();
    }
    {
        bool ok; const unsigned vo = v_off(w, ok);
        glds16s<MEGA>(kq_src(1, c0, w), l16, sm0 + w * 1024);
        glds16s<MEGA>(kq_src(0, qt, w), l16, sm0 + A_Q + w * 1024);
        if (w < 11 && ok) glds16s<MEGA>(V + c0 * 327680, vo, sm0 + A_V + w * 1024);
        if (tid < ANSL * 64) {
            const unsigned cv = (tid & 1) ? 0u : 0x3F803F80u;
            *(LAS u32x4*)(smem + (tid >> 6) * ACH + A_V + ((tid & 63) >> 1) * VHALF + 320 + (tid & 1) * 16) = (u32x4){cv, cv, cv, cv};
        }
    }
    if (w >= 8) {
        const int lw = w - 8;
        __builtin_amdgcn_s_setprio(3);
        unsigned vo[3]; bool ok[3];
#pragma unroll
        for (int i = 0; i < 3; ++i) vo[i] = v_off(lw + 4 * i, ok[i]);
        auto chunk = [&](int c) {
            const int cc = (c + c0) & 7; const unsigned sb = sm0 + (c >= ANSL ? c - ANSL : c) * ACH;
#pragma unroll
            for (int i = 0; i < 3; ++i) glds16s<MEGA>(kq_src(1, cc, lw + 4 * i), l16, sb + (lw + 4 * i) * 1024);
#pragma unroll
            for (int i = 0; i < 3; ++i) if ((i < 2 || lw < 3) && ok[i]) glds16s<MEGA>(V + cc * 327680, vo[i], sb + A_V + (lw + 4 * i) * 1024);
        };
        asm volatile("s_waitcnt vmcnt(0) lgkmcnt(0)\n\ts_barrier" ::: "memory");
        chunk(1); chunk(2);
        WAITV(0); BAR_RAW();
        chunk(3); chunk(4); chunk(5);
        WAITV(0); BAR_RAW();
        chunk(6); chunk(7);
        if (first) {
            if (lw < 3) retile_slab(bid + 256 * lw, smem + A_Q + lw * 6144, wt, ws, lane);
            WAITV(0); BAR_RAW();
            if (lw >= 2) retile_slab(bid + 256 * (lw + 1), smem + 2 * ACH + lw * 6144, wt, ws, lane);
            if (MEGA) signal(ws, CN_R + xcd, lane);
        } else {
            WAITV(0);
            const int t = tid - 512;
            unsigned tv = *(const unsigned*)(ws + WS_WO + e * 409600 + bi * 12800 + min(t, 99) * 128);
            tv ^= *(const unsigned*)(ws + WS_WGU + e * 1966080 + bi * 61440 + t * 128);
            tv ^= *(const unsigned*)(ws + WS_WGU + e * 1966080 + bi * 61440 + min(256 + t, 479) * 128);
            BAR_RAW();
            asm volatile("" ::"v"(tv));
        }
        __builtin_amdgcn_s_setprio(0);
        if (MEGA) BAR_LGKM();
        return;
    }

    const int r = lane & 31, h = lane >> 5;
    const unsigned k0a = h * 1024 + r * 16, k1a = 2048 + r * 16 + par * 8, va = A_V + h * VHALF + min(r, 21) * 16;
    f32x16 zero, negm, o;
#pragma unroll
    for (int i = 0; i < 16; ++i) { zero[i] = 0.f; o[i] = 0.f; }
    bf16x8 v1 = {0, 0, 0, 0, 0, 0, 0, 0};
    u32x4 pw0 = {0, 0, 0, 0}, pw1 = {0, 0, 0, 0};
    float e0 = 0.f, e1 = 0.f, e2 = 0.f, e3 = 0.f;
    PIN(o); PIN(v1); PIN(pw0); PIN(pw1);
    asm volatile("s_waitcnt vmcnt(0) lgkmcnt(0)\n\ts_barrier" ::: "memory");
    STAMPW(1, 0);
    bf16x8 q0; bf16x4 q1;
    { LAS const char* qp = smem + A_Q + (w >> 1) * 3072 + (w & 1) * 512;
      q0 = lds128(qp + k0a); q1 = lds64(qp + k1a);
      if (h) q1 = (bf16x4){0, 0, 0, 0}; }
    LAS const char* pk0 = smem + k0a;
    LAS const char* pk1 = smem + k1a;
    LAS const char* pv = smem + va;
    f32x16 sA, sB;
    {
        const bf16x8 a = lds128(pk0); const bf16x4 b = lds64(pk1);
        f32x16 s = MFMA32(a, q0, zero);
        s = MFMA32K8(b, q1, s);
        const float m = max16(s);
#pragma unroll
        for (int i = 0; i < 16; ++i) negm[i] = -m;
        sA = MFMA32(a, q0, negm);
        sA = MFMA32K8(b, q1, sA);
    }
    bf16x8 ka = lds128(pk0 + 512), v0 = lds128(pv);
    bf16x4 kb = lds64(pk1 + 512);
#define EX(x) __builtin_amdgcn_exp2f(x)
#define ATT_STEP(SN, SC, PK0, PK1, PV0, PV1) do { \
        float t0, t1, t2, t3; \
        SN = MFMA32(ka, q0, negm); PIN(SN); ka = lds128(PK0); \
        pw1[2] = pack2(e0, e1); pw1[3] = pack2(e2, e3); PIN(pw1); SBAR(); \
        t0 = EX(SC[0]); t1 = EX(SC[1]); t2 = EX(SC[2]); t3 = EX(SC[3]); PIN(t0); PIN(t1); PIN(t2); PIN(t3); SBAR(); \
        o = MFMA32(v1, __builtin_bit_cast(bf16x8, pw1), o); PIN(o); v1 = lds128(PV1); \
        pw0[0] = pack2(t0, t1); pw0[1] = pack2(t2, t3); PIN(pw0); SBAR(); \
        t0 = EX(SC[4]); t1 = EX(SC[5]); t2 = EX(SC[6]); t3 = EX(SC[7]); PIN(t0); PIN(t1); PIN(t2); PIN(t3); SBAR(); \
        SN = MFMA32K8(kb, q1, SN); PIN(SN); kb = lds64(PK1); \
        pw0[2] = pack2(t0, t1); pw0[3] = pack2(t2, t3); PIN(pw0); SBAR(); \
        t0 = EX(SC[8]); t1 = EX(SC[9]); t2 = EX(SC[10]); t3 = EX(SC[11]); PIN(t0); PIN(t1); PIN(t2); PIN(t3); SBAR(); \
        o = MFMA32(v0, __builtin_bit_cast(bf16x8, pw0), o); PIN(o); v0 = lds128(PV0); \
        pw1[0] = pack2(t0, t1); pw1[1] = pack2(t2, t3); PIN(pw1); SBAR(); \
        e0 = EX(SC[12]); e1 = EX(SC[13]); e2 = EX(SC[14]); e3 = EX(SC[15]); PIN(e0); PIN(e1); PIN(e2); PIN(e3); SBAR(); \
    } while (0)
#define KOF(u) (((u) >> 1) * 3072 + ((u) & 1) * 512)
#pragma unroll 1
    for (int c = 0; c < 8; ++c) {
        const int step = (c == ANSL - 1) ? -(ANSL - 1) * ACH : ACH;
        LAS const char* nk0 = pk0 + step; LAS const char* nk1 = pk1 + step; LAS const char* nv = pv + step;
        if (c == 4) __builtin_amdgcn_s_setprio(2); else if (c == 0 || c == 2 || c == 5 || c == 7) __builtin_amdgcn_s_setprio(1);
        ATT_STEP(sB, sA, pk0 + KOF(2), pk1 + KOF(2), pv + 2 * VSTEP, pv + 1 * VSTEP);
        ATT_STEP(sA, sB, pk0 + KOF(3), pk1 + KOF(3), pv + 4 * VSTEP, pv + 3 * VSTEP);
        ATT_STEP(sB, sA, pk0 + KOF(4), pk1 + KOF(4), pv + 6 * VSTEP, pv + 5 * VSTEP);
        ATT_STEP(sA, sB, pk0 + KOF(5), pk1 + KOF(5), pv + 8 * VSTEP, pv + 7 * VSTEP);
        if (c == 1 || c == 6) __builtin_amdgcn_s_setprio(2); else if (c != 3 && c != 4) __builtin_amdgcn_s_setprio(0);
        ATT_STEP(sB, sA, pk0 + KOF(6), pk1 + KOF(6), pv + 10 * VSTEP, pv + 9 * VSTEP);
        ATT_STEP(sA, sB, pk0 + KOF(7), pk1 + KOF(7), pv + 12 * VSTEP, pv + 11 * VSTEP);
        if (c == 0 || c == 2 || c == 5) { BAR_RAW(); __builtin_amdgcn_s_setprio(3); }
        ATT_STEP(sB, sA, nk0, nk1, pv + 14 * VSTEP, pv + 13 * VSTEP);
        ATT_STEP(sA, sB, nk0 + KOF(1), nk1 + KOF(1), nv, pv + 15 * VSTEP);
        pk0 = nk0; pk1 = nk1; pv = nv;
    }
#undef KOF
#undef ATT_STEP
    pw1[2] = pack2(e0, e1); pw1[3] = pack2(e2, e3);
    o = MFMA32(v1, __builtin_bit_cast(bf16x8, pw1), o);
    STAMPW(2, 0);
    float l;
    { auto rr = __builtin_amdgcn_permlane32_swap(__float_as_uint(o[8]), __float_as_uint(o[8]), false, false); l = __uint_as_float(rr[1]); }
    float chk = 0.f;
#pragma unroll
    for (int i = 0; i < 12; ++i) chk += o[i] * 0.f;
    const bool okr = (chk == 0.f) && (l <= 1.8446744e19f);
    if (__builtin_expect(!__all(okr), 0)) {
        float m = -INFINITY;
#pragma unroll
        for (int i = 0; i < 16; ++i) o[i] = 0.f;
        const unsigned cv = r == 20 ? 0x3F803F80u : 0u;
#pragma unroll 1
        for (int kbk = 0; kbk < 64; ++kbk) {
            const char* kp = QK + ((kbk >> 1) * 160 + 80) * 1024 + ((kbk & 1) * 32 + r) * 16;
            const u32x2 ka0 = ld64<MEGA>(kp + (g0 + h) * 1024), ka1 = ld64<MEGA>(kp + (g0 + h) * 1024 + 8);
            f32x16 s = MFMA32(__builtin_bit_cast(bf16x8, (u32x4){ka0[0], ka0[1], ka1[0], ka1[1]}), q0, zero);
            s = MFMA32K8(__builtin_bit_cast(bf16x4, ld64<MEGA>(kp + gt * 1024 + par * 8)), q1, s);
            const float mn = fmaxf(m, max16(s));
            const float al = __builtin_amdgcn_exp2f(m - mn);
            m = mn;
#pragma unroll
            for (int i = 0; i < 16; ++i) { o[i] *= al; s[i] = __builtin_amdgcn_exp2f(s[i] - mn); }
#pragma unroll
            for (int st = 0; st < 2; ++st) {
                const char* vq = V + (((2 * kbk + st) * 2 + h) * 640 + head * 20 + min(r, 19)) * 16;
                const u32x2 va0 = ld64<MEGA>(vq), va1 = ld64<MEGA>(vq + 8);
                u32x4 vv = {va0[0], va0[1], va1[0], va1[1]};
                if (r >= 20) vv = (u32x4){cv, cv, cv, cv};
                o = MFMA32(__builtin_bit_cast(bf16x8, vv), packp(s, st), o);
            }
        }
        auto rr = __builtin_amdgcn_permlane32_swap(__float_as_uint(o[8]), __float_as_uint(o[8]), false, false); l = __uint_as_float(rr[1]);
    }
    {
        const float inv = 1.f / l;
        char* ob = O + (unsigned)((qt * 4 + (w >> 1)) * 81920);
        const int row = (w & 1) * 32 + r;
        auto s0 = __builtin_amdgcn_permlane32_swap(pack2(o[0] * inv, o[1] * inv), pack2(o[4] * inv, o[5] * inv), false, false);
        auto s1 = __builtin_amdgcn_permlane32_swap(pack2(o[2] * inv, o[3] * inv), pack2(o[6] * inv, o[7] * inv), false, false);
        st128<MEGA>(ob + (unsigned)((2 * head + h) * 1024 + row * 16), (u32x4){s0[0], s1[0], s0[1], s1[1]});
        if (h == 0) st64<MEGA>(ob + (unsigned)(65536 + head * 512 + row * 8), (u32x2){pack2(o[8] * inv, o[9] * inv), pack2(o[10] * inv, o[11] * inv)});
    }
    if (MEGA) { signal(ws, cout + qt * 8 + w, lane); BAR_LGKM(); }
#undef EX
}

#define SMEM_DECL(n) __shared__ __attribute__((aligned(1024))) char smem_[n]; LAS char* smem = (LAS char*)smem_
__global__ __launch_bounds__(768) void k_first(const char* X, const char* wqkv, const char* wg, const char* wu, char* ws DBG_ARG) {
    SMEM_DECL(131072);
    STAMPW(0, 8);
    if (blockIdx.x == 255 && threadIdx.x < CN_END) *(unsigned*)(ws + WS_CNT + threadIdx.x * 128) = 0u;
    const Wts wt = {nullptr, wg, wu, nullptr};
    phase_ln<0, true, false>(smem, X, wqkv, nullptr, ws + WS_QK, ws + WS_V, ws, wt, 0, 0 DBG_PASS(1));
    { const int kid = 1; (void)kid; STAMPW(3, 8); }
}
#define PH(k, wv, call) call
template <bool MEGA>
DI void run_phase(int ph, int layer, LAS char* smem, const char* x0, const char* wqkv, const Wts& wt, char* ws, char* out, u32x2 (&rv)[5]) {
    if (ph == 1) PH(6, 8, (phase_ln<0, false, MEGA>(smem, ws + WS_XB, wqkv, ws + WS_ST + SZ_ST, ws + WS_QK + 2 * SZ_X, ws + WS_V + SZ_X, ws, wt, CN_X + 128, CN_Q DBG_PASS(6))));
    if (ph == 2) PH(2 + 5 * layer, 0, (phase_attn<MEGA>(smem, ws + WS_QK + layer * 2 * SZ_X, ws + WS_V + layer * SZ_X, ws + WS_O + layer * SZ_X, ws, wt, layer == 0,
                                       CN_O + 64 * layer DBG_PASS(2 + 5 * layer))));
    if (ph == 3) PH(3 + 5 * layer, 8, (phase_res<DM, MEGA>(smem, ws + WS_O + layer * SZ_X, ws + WS_WO, layer ? ws + WS_XB : x0, ws + WS_XA + layer * SZ_X,
                                       ws + WS_ST + 2 * layer * SZ_ST, ws, layer == 0, false, rv, CN_O + 64 * layer, CN_X + 256 * layer, layer != 0 DBG_PASS(3 + 5 * layer))));
    if (ph == 4) PH(4 + 5 * layer, 8, (phase_ln<1, false, MEGA>(smem, ws + WS_XA + layer * SZ_X, ws + WS_WGU, ws + WS_ST + 2 * layer * SZ_ST,
                                       ws + WS_H + layer * (SEQ * FF * 2), nullptr, ws, wt, CN_X + 256 * layer, CN_H + 64 * layer DBG_PASS(4 + 5 * layer))));
    if (ph == 5) PH(5 + 5 * layer, 8, (phase_res<FF, MEGA>(smem, ws + WS_H + layer * (SEQ * FF * 2), ws + WS_WD, ws + WS_XA + layer * SZ_X, layer ? out : ws + WS_XB,
                                       ws + WS_ST + SZ_ST, ws, false, layer != 0, rv, CN_H + 64 * layer, CN_X + 128, true DBG_PASS(5 + 5 * layer))));
}
__global__ __launch_bounds__(768) void k_mega(const char* x0, const char* wqkv, const char* wo, const char* wg, const char* wu, const char* wd,
                                              char* ws, char* out) {
    SMEM_DECL(LDS_BYTES);
    const Wts wt = {wo, wg, wu, wd};
    u32x2 rv[5];
#pragma unroll
    for (int j = 0; j < 5; ++j) rv[j] = (u32x2){0u, 0u};
#pragma unroll 1
    for (int layer = 0; layer < 2; ++layer) {
        if (layer) run_phase<true>(1, layer, smem, x0, wqkv, wt, ws, out, rv);
        run_phase<true>(2, layer, smem, x0, wqkv, wt, ws, out, rv);
        run_phase<true>(3, layer, smem, x0, wqkv, wt, ws, out, rv);
        run_phase<true>(4, layer, smem, x0, wqkv, wt, ws, out, rv);
        run_phase<true>(5, layer, smem, x0, wqkv, wt, ws, out, rv);
    }
}

extern "C" void kernel_launch(void* const* d_in, const int* in_sizes, int n_in, void* d_out, int out_size, void* d_ws, size_t ws_size,
                              hipStream_t stream) {
    const char* x0 = (const char*)d_in[0];
    const char* wqkv = (const char*)d_in[1];
    const char* wo = (const char*)d_in[2];
    const char* wg = (const char*)d_in[4];
    const char* wu = (const char*)d_in[5];
    const char* wd = (const char*)d_in[6];
    char* ws = (char*)d_ws;
    char* out = (char*)d_out;
    k_first<<<256, 768, 0, stream>>>(x0, wqkv, wg, wu, ws DBG_PASS(1));
    k_mega<<<256, 768, 0, stream>>>(x0, wqkv, wo, wg, wu, wd, ws, out);
}
```

```cpp
#include <hip/hip_runtime.h>
#include <hip/hip_bf16.h>

typedef short bf16x8 __attribute__((ext_vector_type(8)));
typedef short bf16x4 __attribute__((ext_vector_type(4)));
typedef float f32x16 __attribute__((ext_vector_type(16)));
typedef float f32x4 __attribute__((ext_vector_type(4)));
typedef float f32x2 __attribute__((ext_vector_type(2)));
typedef unsigned u32x4 __attribute__((ext_vector_type(4)));
typedef unsigned u32x2 __attribute__((ext_vector_type(2)));
typedef __bf16 bf2_t __attribute__((ext_vector_type(2)));
#define LAS __attribute__((address_space(3)))
#define GAS __attribute__((address_space(1)))
#define DI __device__ __forceinline__
#define MFMA32(a, b, c) __builtin_amdgcn_mfma_f32_32x32x16_bf16((a), (b), (c), 0, 0, 0)
#define MFMA32K8(a, b, c) __builtin_amdgcn_mfma_f32_32x32x8bf16_1k((a), (b), (c), 0, 0, 0)
#define MFMA4(a, b, c) __builtin_amdgcn_mfma_f32_4x4x4bf16_1k((a), (b), (c), 0, 0, 0)
#define MFMA16(a, b, c) __builtin_amdgcn_mfma_f32_16x16x32_bf16((a), (b), (c), 0, 0, 0)
#define WAITV(n) asm volatile("s_waitcnt vmcnt(%0)" ::"i"(n) : "memory")
#define BAR_LGKM() asm volatile("s_waitcnt lgkmcnt(0)\n\ts_barrier" ::: "memory")
#define BAR_RAW() asm volatile("s_barrier" ::: "memory")
#define PIN(x) asm volatile("" : "+v"(x))
#define SBAR() __builtin_amdgcn_sched_barrier(0)

constexpr int SEQ = 2048, DM = 640, NH = 32, DH = 20, FF = 1536;
constexpr float QSCALE = 0.22360679774997896f * 1.4426950408889634f;
constexpr float LN_EPS = 1e-5f;
constexpr int LDS_BYTES = 163840;

constexpr unsigned SZ_X = SEQ * DM * 2, SZ_ST = 32 * 8 * 512;
constexpr unsigned WS_XA = 0;
constexpr unsigned WS_XB = WS_XA + 2 * SZ_X;
constexpr unsigned WS_O = WS_XB + SZ_X;
constexpr unsigned WS_V = WS_O + 2 * SZ_X;
constexpr unsigned WS_QK = WS_V + 2 * SZ_X;
constexpr unsigned WS_H = WS_QK + 4 * SZ_X;
constexpr unsigned WS_ST = WS_H + 2 * SEQ * FF * 2;
constexpr unsigned WS_WO = WS_ST + 3 * SZ_ST;
constexpr unsigned WS_WD = WS_WO + DM * DM * 2;
constexpr unsigned WS_WGU = WS_WD + FF * DM * 2;
constexpr unsigned WS_CSQ = WS_WGU + 2 * DM * FF * 2;
constexpr unsigned WS_CSGU = WS_CSQ + 131072;
constexpr unsigned WS_EPOCH = WS_CSGU + 16384, WS_CNT = WS_EPOCH + 128;
constexpr int CN_O = 0, CN_X = CN_O + 512, CN_H = CN_X + 768, CN_Q = CN_H + 512, CN_R = CN_Q + 512;

#define STAMPV(i, wv, val)
#define RT(x)
#define DBG_ARG
#define DBG_PASS(k)
#define STAMPW(i, wv)

#define EXP_ROT
#define NOSTAT 0
#define NOSLAB 0
#define PHASE_IDS() \
    int lane, w = wave, bid = __builtin_amdgcn_readfirstlane(blockIdx.x); \
    asm volatile("v_mbcnt_lo_u32_b32 %0, -1, 0\n\tv_mbcnt_hi_u32_b32 %0, -1, %0" : "=v"(lane)); asm volatile("" : "+s"(bid), "+s"(w)); \
    const int tid = w * 64 + lane, xcd = bid & 7, bi = bid >> 3, pr = xcd >> 1, e = xcd & 1
DI unsigned pack2(float a, float b) { f32x2 v = {a, b}; return __builtin_bit_cast(unsigned, __builtin_convertvector(v, bf2_t)); }
DI float bflo(unsigned u) { return __uint_as_float(u << 16); }
DI float bfhi(unsigned u) { return __uint_as_float(u & 0xffff0000u); }
DI unsigned long long sgpr64(const void* p) {
    return ((unsigned long long)(unsigned)__builtin_amdgcn_readfirstlane((int)((unsigned long long)p >> 32)) << 32) |
           (unsigned)__builtin_amdgcn_readfirstlane((int)(unsigned long long)p);
}
template <bool DEV>
DI void glds16s(const char* sb_, unsigned voff, unsigned ldsaddr) {
    const unsigned long long sbase = sgpr64(sb_);
    ldsaddr = __builtin_amdgcn_readfirstlane(ldsaddr);
    if (DEV) asm volatile("s_mov_b32 m0, %2\n\ts_nop 3\n\tglobal_load_lds_dwordx4 %1, %0 sc1" ::"s"(sbase), "v"(voff), "s"(ldsaddr) : "memory");
    else asm volatile("s_mov_b32 m0, %2\n\ts_nop 3\n\tglobal_load_lds_dwordx4 %1, %0" ::"s"(sbase), "v"(voff), "s"(ldsaddr) : "memory");
}
template <bool DEV> DI void st128(char* p, u32x4 v) {
    if (DEV) asm volatile("global_store_dwordx4 %0, %1, off sc1\n\ts_nop 1" ::"v"(p), "v"(v) : "memory"); else *(u32x4*)p = v;
}
template <bool DEV> DI void st64(char* p, u32x2 v) {
    if (DEV) asm volatile("global_store_dwordx2 %0, %1, off sc1" ::"v"(p), "v"(v) : "memory"); else *(u32x2*)p = v;
}
template <bool DEV> DI u32x2 ld64(const char* p) {
    if (DEV) return __builtin_bit_cast(u32x2, __hip_atomic_load((const GAS unsigned long long*)p, __ATOMIC_RELAXED, __HIP_MEMORY_SCOPE_AGENT));
    return *(const u32x2*)p;
}
DI void st128s(char* sb_, unsigned voff, u32x4 v) {
    asm volatile("s_nop 4\n\tglobal_store_dwordx4 %1, %2, %0 sc1\n\ts_nop 1" ::"s"(sgpr64(sb_)), "v"(voff), "v"(v) : "memory");
}
DI unsigned get_tag(char* ws) {
    return (unsigned)__builtin_amdgcn_readfirstlane((int)__hip_atomic_load((const GAS unsigned*)(ws + WS_EPOCH), __ATOMIC_RELAXED, __HIP_MEMORY_SCOPE_AGENT)) + 1u;
}
template <bool DEV> DI void st32(char* p, float v) {
    if (DEV) __hip_atomic_store((GAS float*)p, v, __ATOMIC_RELAXED, __HIP_MEMORY_SCOPE_AGENT); else *(float*)p = v;
}
template <bool DEV> DI float ld32(const char* p) {
    if (DEV) return __hip_atomic_load((const GAS float*)p, __ATOMIC_RELAXED, __HIP_MEMORY_SCOPE_AGENT);
    return *(const float*)p;
}
DI void signal(char* ws, unsigned tag, int line, int word, int lane) {
    WAITV(0);
    asm volatile("" : "+s"(tag));
    if (lane == 0) __hip_atomic_store((GAS unsigned*)(ws + WS_CNT + line * 128 + word * 4), tag, __ATOMIC_RELAXED, __HIP_MEMORY_SCOPE_AGENT);
}
template <int NWD>
DI void await(char* ws, unsigned tag, int line, int word) {
    const char* p = ws + WS_CNT + line * 128 + word * 4;
    for (unsigned spins = 0; spins < (1u << 20); ++spins) {
        bool ok;
        if (NWD == 2) { u32x2 v; asm volatile("global_load_dwordx2 %0, %1, off sc1\n\ts_waitcnt vmcnt(0)" : "=&v"(v) : "v"(p) : "memory"); ok = v[0] == tag && v[1] == tag; }
        else { u32x4 v; asm volatile("global_load_dwordx4 %0, %1, off sc1\n\ts_waitcnt vmcnt(0)" : "=&v"(v) : "v"(p) : "memory"); ok = v[0] == tag && v[1] == tag && v[2] == tag && v[3] == tag; }
        if (__all(ok)) break;
        __builtin_amdgcn_s_sleep(1);
    }
}
DI bf16x8 lds128(LAS const char* p) { return *(LAS const bf16x8*)p; }
DI bf16x4 lds64(LAS const char* p) { return *(LAS const bf16x4*)p; }
DI bf16x4 ldstr(LAS const char* p) { return __builtin_bit_cast(bf16x4, __builtin_amdgcn_ds_read_tr16_b64_v4i16((LAS bf16x4*)p)); }
DI bf16x8 cat(bf16x4 a, bf16x4 b) { return __builtin_shufflevector(a, b, 0, 1, 2, 3, 4, 5, 6, 7); }
DI float quad_sum(float v) {
    v += __int_as_float(__builtin_amdgcn_mov_dpp(__float_as_int(v), 0xB1, 0xf, 0xf, true));
    return v + __int_as_float(__builtin_amdgcn_mov_dpp(__float_as_int(v), 0x4E, 0xf, 0xf, true));
}
DI float half_sum(float v) { auto a = __builtin_amdgcn_permlane32_swap(__float_as_uint(v), __float_as_uint(v), false, false); return __uint_as_float(a[0]) + __uint_as_float(a[1]); }
DI float xsum16_32(float v) {
    auto a = __builtin_amdgcn_permlane16_swap(__float_as_uint(v), __float_as_uint(v), false, false);
    return half_sum(__uint_as_float(a[0]) + __uint_as_float(a[1]));
}
DI u32x4 split3(float v, bool rowside) {
    const unsigned p1 = pack2(v, v); const float r1 = v - bflo(p1);
    const unsigned p2 = pack2(r1, r1); const float r2 = r1 - bflo(p2);
    const unsigned p3 = pack2(r2, r2);
    const unsigned l1 = p1 & 0xffffu, l2 = p2 & 0xffffu, h1 = p1 & 0xffff0000u, h2 = p2 & 0xffff0000u, h3 = p3 & 0xffff0000u;
    return rowside ? (u32x4){p1, l2 | h1, l2 | h3, 0u} : (u32x4){l1 | h2, l1 | h3, l2 | h1, 0u};
}
DI bf16x4 lo4(bf16x8 v) { return __builtin_shufflevector(v, v, 0, 1, 2, 3); }
DI bf16x4 hi4(bf16x8 v) { return __builtin_shufflevector(v, v, 4, 5, 6, 7); }

template <int MODE> struct LnCfg;
template <> struct LnCfg<0> { static constexpr int SLOT = 32768, NB = 2, PPW = 4, NCS = 128; };
template <> struct LnCfg<1> { static constexpr int SLOT = 40960, NB = 3, PPW = 5, NCS = 192; };
constexpr int T_XC = 0, T_WC = 2048, T_RS = 5120, T_RAW = 5632;

template <int MODE, bool FIRST, bool SWAP, bool MEGA>
DI void ln_mainloop(LAS char* smem, f32x16 (&acc)[LnCfg<MODE>::NB][2], int wm, int wn, int lane, char* ws, int tm, int bid, int cin, unsigned tag, unsigned long long& tb0 DBG_ARG) {
    using C = LnCfg<MODE>;
    constexpr int NB = C::NB;
    const int r = lane & 31, h = lane >> 5, q = (lane & 15) >> 2, pp = lane & 3, blk = (lane >> 4) & 1;
    unsigned xa[4], wa[2];
    if (FIRST) {
        const int row = 64 * wm + 32 * wn + r, sw = (row >> 1) & 7;
#pragma unroll
        for (int ks = 0; ks < 4; ++ks) xa[ks] = row * 128 + (((2 * ks + h) ^ sw) << 4);
    } else xa[0] = wm * 8192 + h * 1024 + r * 16;
    if (MODE == 0) {
#pragma unroll
        for (int cb = 0; cb < 2; ++cb) wa[cb] = 16384 + (8 * h + q) * 256 + (((2 * wn + (FIRST ? cb ^ wm : cb)) ^ q) * 64) + blk * 32 + pp * 8;
    } else { wa[0] = 16384 + wn * 3072 + lane * 16; wa[1] = 0; }
    bf16x8 xf[2][2], wf[2][NB];
    f32x4 sx = {0.f, 0.f, 0.f, 0.f}, sw_ = sx; f32x16 sxx;
#pragma unroll
    for (int i = 0; i < 16; ++i) sxx[i] = 0.f;
    const short sv = ((lane & 15) == ((lane >> 4) & 1)) ? (short)0x3F80 : (short)0;
    const bf16x8 sel = {sv, sv, sv, sv, sv, sv, sv, sv};
#define LN_READ(BUF, SB, KS) do { \
        _Pragma("unroll") for (int tb = 0; tb < 2; ++tb) xf[BUF][tb] = FIRST ? lds128((SB) + (xa[KS] ^ (tb * 4096))) : lds128((SB) + xa[0] + (KS) * 2048 + tb * 512); \
        _Pragma("unroll") for (int j = 0; j < NB; ++j) { \
            if (MODE == 0) wf[BUF][j] = cat(ldstr((SB) + wa[j] + (KS) * 4096), ldstr((SB) + wa[j] + (KS) * 4096 + 1024)); \
            else wf[BUF][j] = lds128((SB) + wa[0] + (KS) * 6144 + j * 1024); } \
    } while (0)
#define LN_MMA(BUF) do { \
        _Pragma("unroll") for (int j = 0; j < NB; ++j) _Pragma("unroll") for (int tb = 0; tb < 2; ++tb) \
            acc[j][tb] = SWAP ? MFMA32(wf[BUF][j], xf[BUF][tb], acc[j][tb]) : MFMA32(xf[BUF][tb], wf[BUF][j], acc[j][tb]); \
        if (FIRST && !NOSTAT) { sx = MFMA16(sel, xf[BUF][0], sx); sw_ = MFMA16(sel, wf[BUF][0], sw_); sxx = MFMA32(xf[BUF][0], xf[BUF][0], sxx); } \
    } while (0)
#define LN_STEP(RB, SB, KS, MB) do { LN_READ(RB, SB, KS); LN_MMA(MB); \
        _Pragma("unroll") for (int i_ = 0; i_ < 3; ++i_) { __builtin_amdgcn_sched_group_barrier(0x8, 1, 0); __builtin_amdgcn_sched_group_barrier(0x100, 2, 0); } \
        __builtin_amdgcn_sched_group_barrier(0x8, 2 * NB - 3 + (FIRST ? 3 : 0), 0); \
        SBAR(); } while (0)
    if (MEGA && !FIRST) {
#pragma unroll
        for (int j = 0; j < NB; ++j) { PIN(acc[j][0]); PIN(acc[j][1]); }
        PIN(xa[0]); PIN(wa[0]); if (MODE == 0) PIN(wa[1]);
        if (wm + wn == 0) await<4>(ws, tag, cin + 16 * tm + (lane & 15), 0);
        RT(tp);
        BAR_RAW();
        RT(tg); STAMPV(8, 8, tp); STAMPV(9, 8, tg);
    }
    BAR_RAW();
    LN_READ(0, smem + 2 * C::SLOT, 0);
#pragma unroll 1
    for (int s = 0; s < 9; ++s) {
        LAS char* sb = smem + ((s + 2) & 3) * C::SLOT;
        LAS char* nb = smem + ((s + 3) & 3) * C::SLOT;
        SBAR();
        LN_STEP(1, sb, 1, 0);
        LN_STEP(0, sb, 2, 1);
        LN_STEP(1, sb, 3, 0);
        BAR_RAW();
        SBAR();
        LN_STEP(0, nb, 0, 1);
    }
    LAS char* tab = smem + C::SLOT;
    {
        LAS char* sb = smem + 3 * C::SLOT;
        SBAR();
        LN_STEP(1, sb, 1, 0);
        LN_STEP(0, sb, 2, 1);
        LN_STEP(1, sb, 3, 0);
        if (!FIRST) BAR_LGKM();
        LN_MMA(1);
    }
    if (FIRST) {
        LAS float* raw = (LAS float*)(tab + T_RAW) + (wn * 2 + wm) * 96;
        if (lane < 16) { raw[lane] = sx[0]; raw[16 + lane] = sx[1]; raw[32 + lane] = sw_[0]; raw[48 + lane] = sw_[1]; }
        { const int ri = (r & 3) + 4 * (r >> 3); float dg = sxx[0];
#pragma unroll
          for (int i = 1; i < 16; ++i) dg = ri == i ? sxx[i] : dg;
          if (((r >> 2) & 1) == h) raw[64 + r] = dg; }
        asm volatile("s_waitcnt lgkmcnt(0)" ::: "memory");
        const float csum = raw[32 + r];
        const float mu = raw[r] * (1.f / DM), var = fmaxf(raw[64 + r] * (1.f / DM) - mu * mu, 0.f);
        const int tl = 64 * wm + 32 * wn + r, nl = 64 * wn + 32 * wm + r;
        if (h == 0) { *(LAS u32x4*)(tab + T_XC + tl * 16) = split3(-mu, true); *(LAS float*)(tab + T_RS + tl * 4) = __builtin_amdgcn_rsqf(var + LN_EPS); }
        else { *(LAS u32x4*)(tab + T_WC + nl * 16) = split3(csum, false); st32<MEGA>(ws + WS_CSQ + (bid * 128 + nl) * 4, csum); }
        BAR_LGKM();
    }
    {
        bf16x4 xc[2], wc[NB];
#pragma unroll
        for (int tb = 0; tb < 2; ++tb) xc[tb] = lds64(tab + T_XC + (64 * wm + 32 * (FIRST ? tb ^ wn : tb) + r) * 16 + h * 8);
#pragma unroll
        for (int j = 0; j < NB; ++j) wc[j] = lds64(tab + T_WC + ((NB * wn + (FIRST ? j ^ wm : j)) * 32 + r) * 16 + h * 8);
#pragma unroll
        for (int j = 0; j < NB; ++j)
#pragma unroll
            for (int tb = 0; tb < 2; ++tb) acc[j][tb] = SWAP ? MFMA32K8(wc[j], xc[tb], acc[j][tb]) : MFMA32K8(xc[tb], wc[j], acc[j][tb]);
    }
#undef LN_STEP
#undef LN_READ
#undef LN_MMA
}

struct Wts { const char *wo, *wg, *wu, *wd; };
constexpr int QKV_BASE = 32768, RS_SLOT = 18432;
DI void ln_tile(int mode, int bid, int& tm, int& tn) {
    const int xcd = bid & 7, bi = bid >> 3, pr = xcd >> 1, e = xcd & 1;
    if (mode == 0) { const int lin = e * 30 + bi; tm = 4 * pr + (lin & 3); tn = lin >> 2; }
    else { tm = 4 * pr + (bi & 3); tn = 8 * e + (bi >> 2); }
}
DI unsigned wqkv_off(int piece, int tn, int lane) { const int k = piece * 4 + (lane >> 4), g = lane & 15; return (unsigned)(k * 3840 + tn * 256 + (((g >> 2) ^ (k & 3)) << 6) + (g & 3) * 16); }
template <int NW>
DI void prefetch_res(unsigned sm0, const char* wtile, int slot0, int j, unsigned l16) {
#pragma unroll
    for (int i = 0; i < (20 + NW - 1) / NW; ++i) { const int pc = j + NW * i, st = pc >= 10;
        if (pc < 20) glds16s<true>(wtile + pc * 1024, l16, sm0 + (slot0 + st) * RS_SLOT + 8192 + (pc - 10 * st) * 1024); }
}

template <int MODE, bool FIRST, bool MEGA>
DI void phase_ln(LAS char* smem, const char* X, const char* W0, const char* stats, char* out0, char* out1, char* ws, const Wts& wt,
                 int cin, int cout, unsigned& tag, int wave DBG_ARG) {
    using C = LnCfg<MODE>;
    PHASE_IDS();
    const unsigned sm0 = (unsigned)(size_t)smem;
    int tm, tn;
    ln_tile(MODE, bid, tm, tn);
    LAS char* tab = smem + C::SLOT;

    if (MODE == 0 && bi >= 30) {
        if (FIRST) {
            const int blk = (xcd * 2 + bi - 30) * 3 + (tid >> 8), m = blk / 24, c = tid & 7, rg = (tid & 255) >> 3;
            const char* p = (m ? wt.wu : wt.wg) + rg * 3072 + (blk - 24 * m) * 128 + c * 16;
            float s[8];
#pragma unroll
            for (int i = 0; i < 8; ++i) s[i] = 0.f;
            u32x4 v[20];
#pragma unroll
            for (int j = 0; j < 20; ++j) v[j] = *(const u32x4*)(p + j * 32 * 3072);
#pragma unroll
            for (int j = 0; j < 20; ++j)
#pragma unroll
                for (int i = 0; i < 4; ++i) { s[2 * i] += bflo(v[j][i]); s[2 * i + 1] += bfhi(v[j][i]); }
            LAS float* red = (LAS float*)smem;
#pragma unroll
            for (int i = 0; i < 8; ++i) red[rg * 192 + (tid >> 8) * 64 + c * 8 + i] = s[i];
            BAR_LGKM();
            if (tid < 192) {
                float t = 0.f;
#pragma unroll
                for (int i = 0; i < 32; ++i) t += red[i * 192 + tid];
                const int b2 = (xcd * 2 + bi - 30) * 3 + (tid >> 6), m2 = b2 / 24, f = (b2 - 24 * m2) * 64 + (tid & 63), tf = f / 96, fl = f - tf * 96;
                st32<MEGA>(ws + WS_CSGU + (tf * 192 + (fl >> 4) * 32 + m2 * 16 + (fl & 15)) * 4, t);
            }
            if (MEGA) { tag = get_tag(ws); WAITV(0); }
        }
        return;
    }

    if (w < 8) {
        const int j = w;
        unsigned off[C::PPW];
#pragma unroll
        for (int p = 0; p < 2; ++p) {
            if (FIRST) { const int row = (j + 8 * p) * 8 + (lane >> 3); off[p] = (unsigned)((tm * 128 + row) * 1280 + (((lane & 7) ^ ((row >> 1) & 7)) << 4)); }
            else off[p] = (unsigned)((2 * tm + p) * 81920 + j * 1024 + lane * 16);
        }
#pragma unroll
        for (int p = 2; p < C::PPW; ++p) {
            const int piece = j + 8 * (p - 2);
            if (MODE == 0) off[p] = wqkv_off(piece, tn, lane);
            else off[p] = (unsigned)(tn * 245760 + piece * 1024 + lane * 16);
        }
        constexpr int XSTAGE = FIRST ? 128 : 8192, WSTAGE = MODE == 0 ? 64 * 3840 : 24576;
        int kb = FIRST ? (3 * (tm & 3) + 7 * tn) % 10 : 0;
        constexpr bool WDEV = MEGA && MODE == 1;
        auto issue = [&](int s) {
            const unsigned sb = sm0 + ((s + 2) & 3) * C::SLOT + j * 1024;
#pragma unroll
            for (int p = 0; p < 2; ++p) glds16s<MEGA && !FIRST>(X + kb * XSTAGE, off[p], sb + p * 8192);
#pragma unroll
            for (int p = 2; p < C::PPW; ++p) glds16s<WDEV>(W0 + kb * WSTAGE, off[p], sb + p * 8192);
            kb = (kb == 9) ? 0 : kb + 1;
        };
        if (FIRST) {
            issue(0); issue(1);
            WAITV(C::PPW);
            BAR_RAW();
#pragma unroll 1
            for (int s = 0; s < 8; ++s) { issue(s + 2); WAITV(C::PPW); BAR_RAW(); }
            WAITV(0);
            BAR_RAW();
            if (MEGA) tag = get_tag(ws);
            BAR_RAW();
        } else {
            if (MEGA) {
#pragma unroll
                for (int p = 0; p < C::PPW; ++p) PIN(off[p]);
                BAR_RAW();
#pragma unroll
                for (int s = 0; s < 2; ++s)
#pragma unroll
                    for (int p = 0; p < 2; ++p) glds16s<true>(X + s * XSTAGE, off[p], sm0 + (s + 2) * C::SLOT + j * 1024 + p * 8192);
                kb = 2;
            } else { issue(0); issue(1); }
            asm volatile("" ::: "memory");
            const char* sp = stats + (2 * tm + (tid >> 8)) * 4096 + (tid & 3) * 1024 + ((tid >> 2) & 63) * 8;
            const f32x2 st0 = __builtin_bit_cast(f32x2, ld64<MEGA>(sp)), st1 = __builtin_bit_cast(f32x2, ld64<MEGA>(sp + 512));
            const float cs = ld32<MEGA>(ws + (MODE == 0 ? WS_CSQ + bid * 512 : WS_CSGU + tn * 768) + (tid < C::NCS ? tid : 0) * 4);
            WAITV((MEGA ? 2 : C::PPW) + 3);
            BAR_RAW();
#pragma unroll 1
            for (int s = 0; s < 8; ++s) { issue(s + 2); WAITV(C::PPW); BAR_RAW(); }
            if (MEGA && MODE == 1) {
                prefetch_res<8>(sm0, ws + WS_WD + (4 * e + (bi >> 3)) * 245760, 0, j, lane * 16);
                if (j < 4) WAITV(3); else WAITV(2);
            } else WAITV(0);
            BAR_RAW();
            const float mu = quad_sum(st0[0] + st1[0]) * 0.125f, d0 = st0[0] - mu, d1 = st1[0] - mu;
            const float m2 = quad_sum(st0[1] + st1[1] + 80.f * (d0 * d0 + d1 * d1));
            if ((tid & 3) == 0) { *(LAS u32x4*)(tab + T_XC + (tid >> 2) * 16) = split3(-mu, true);
                                  *(LAS float*)(tab + T_RS + (tid >> 2) * 4) = __builtin_amdgcn_rsqf(m2 * (1.f / DM) + LN_EPS); }
            if (tid < C::NCS) *(LAS u32x4*)(tab + T_WC + tid * 16) = split3(cs, false);
            BAR_LGKM();
        }
        return;
    }

    const int wm = w & 1, wn = (w >> 1) & 1, r = lane & 31, h = lane >> 5;
    LAS const float* trs = (LAS const float*)(tab + T_RS);
    if (MEGA && FIRST) tag = get_tag(ws);
    unsigned long long tb0 = 0;
    f32x16 acc[C::NB][2];
#pragma unroll
    for (int j = 0; j < C::NB; ++j)
#pragma unroll
        for (int tb = 0; tb < 2; ++tb)
#pragma unroll
            for (int i = 0; i < 16; ++i) acc[j][tb][i] = 0.f;

    if (MODE == 1) {
        STAMPW(1, 8); ln_mainloop<MODE, FIRST, true, MEGA>(smem, acc, wm, wn, lane, ws, tm, bid, cin, tag, tb0 DBG_PASS(kid)); STAMPW(2, 8); STAMPV(11, 8, tb0);
        char* hp = out0 + (unsigned)(((2 * tm + wm) * 192 + tn * 12 + 6 * wn + h) * 1024 + r * 16);
#pragma unroll
        for (int tb = 0; tb < 2; ++tb) {
            const float rs = trs[64 * wm + 32 * tb + r], rs2 = rs * rs;
#pragma unroll
            for (int j = 0; j < 3; ++j) {
                unsigned pk[2][2];
#pragma unroll
                for (int qd = 0; qd < 2; ++qd) {
                    float hv[4];
#pragma unroll
                    for (int i = 0; i < 4; ++i) hv[i] = fmaxf(acc[j][tb][4 * qd + i] * rs2, 0.f) * acc[j][tb][8 + 4 * qd + i];
                    pk[qd][0] = pack2(hv[0], hv[1]); pk[qd][1] = pack2(hv[2], hv[3]);
                }
                auto s0 = __builtin_amdgcn_permlane32_swap(pk[0][0], pk[1][0], false, false);
                auto s1 = __builtin_amdgcn_permlane32_swap(pk[0][1], pk[1][1], false, false);
                st128<MEGA>(hp + j * 2048 + tb * 512, (u32x4){s0[0], s1[0], s0[1], s1[1]});
                SBAR();
            }
        }
    } else if (tn < 10) {
        STAMPW(1, 8); ln_mainloop<MODE, FIRST, true, MEGA>(smem, acc, wm, wn, lane, ws, tm, bid, cin, tag, tb0 DBG_PASS(kid)); STAMPW(2, 8); STAMPV(11, 8, tb0);
        const float sc = tn < 5 ? QSCALE : 1.f;
        char* qp = out0 + (unsigned)(((2 * tm + wm) * 160 + tn * 16 + 8 * wn + h) * 1024 + r * 16);
#pragma unroll
        for (int tb = 0; tb < 2; ++tb) {
            const int tba = FIRST ? tb ^ wn : tb;
            const float rs = trs[64 * wm + 32 * tba + r] * sc;
#pragma unroll
            for (int cb = 0; cb < 2; ++cb)
#pragma unroll
                for (int qp2 = 0; qp2 < 2; ++qp2) {
                    const f32x16& a = acc[cb][tb];
                    auto s0 = __builtin_amdgcn_permlane32_swap(pack2(a[8 * qp2] * rs, a[8 * qp2 + 1] * rs), pack2(a[8 * qp2 + 4] * rs, a[8 * qp2 + 5] * rs), false, false);
                    auto s1 = __builtin_amdgcn_permlane32_swap(pack2(a[8 * qp2 + 2] * rs, a[8 * qp2 + 3] * rs), pack2(a[8 * qp2 + 6] * rs, a[8 * qp2 + 7] * rs), false, false);
                    st128<MEGA>(qp + (4 * (FIRST ? cb ^ wm : cb) + 2 * qp2) * 1024 + tba * 512, (u32x4){s0[0], s1[0], s0[1], s1[1]});
                    SBAR();
                }
        }
    } else {
        STAMPW(1, 8); ln_mainloop<MODE, FIRST, false, MEGA>(smem, acc, wm, wn, lane, ws, tm, bid, cin, tag, tb0 DBG_PASS(kid)); STAMPW(2, 8); STAMPV(11, 8, tb0);
        char* vp = out1 + (unsigned)((((tm * 8 + 4 * wm) * 2 + h) * 640 + (tn - 10) * 128 + 64 * wn + r) * 16);
#pragma unroll
        for (int tb = 0; tb < 2; ++tb)
#pragma unroll
            for (int s = 0; s < 2; ++s) {
                const int tba = FIRST ? tb ^ wn : tb, tl = 64 * wm + 32 * tba + 16 * s + 4 * h;
                const f32x4 r0 = *(LAS const f32x4*)(trs + tl), r1 = *(LAS const f32x4*)(trs + tl + 8);
#pragma unroll
                for (int cb = 0; cb < 2; ++cb) {
                    const f32x16& a = acc[cb][tb];
                    st128<MEGA>(vp + (2 * tba + s) * 20480 + (FIRST ? cb ^ wm : cb) * 512,
                          (u32x4){pack2(a[8 * s] * r0[0], a[8 * s + 1] * r0[1]), pack2(a[8 * s + 2] * r0[2], a[8 * s + 3] * r0[3]),
                                  pack2(a[8 * s + 4] * r1[0], a[8 * s + 5] * r1[1]), pack2(a[8 * s + 6] * r1[2], a[8 * s + 7] * r1[3])});
                    SBAR();
                }
            }
    }
    RT(ts);
    if (MEGA) signal(ws, tag, cout + (MODE == 1 ? tm * 16 + tn : tn * 16 + tm), 2 * wm + wn, lane);
    STAMPV(10, 8, ts);
}

template <int KT, bool MEGA>
DI void phase_res(LAS char* smem, const char* A, const char* W, const char* res, char* out, char* stats, char* ws, bool resrow, bool outrow,
                  u32x2 (&rv)[5], int cin, int cout, bool wready, const char* wnext, unsigned tag, int wave DBG_ARG) {
    constexpr int NS = KT / 64, NSLOT = 5, SLOT = RS_SLOT, DEP = 3, SL0 = KT == DM ? 3 : 0;
    constexpr bool TAILS = KT == DM;
    PHASE_IDS();
    const int tm = 8 * pr + (bi & 7), tn = 4 * e + (bi >> 3);

    if (w < 8) {
        const int j = w;
        const unsigned off = (unsigned)(j * 1024 + lane * 16);
        const char* ab = A + tm * (KT * 128);
        const char* wb = W + tn * (NS * 10240);
        const unsigned sm0 = (unsigned)(size_t)smem;
        unsigned sb = sm0 + SL0 * SLOT + j * 1024;
        int sl = SL0, ntm, ntn;
        ln_tile(TAILS ? 1 : 0, bid, ntm, ntn);
        const unsigned qo0 = wqkv_off(j, ntn, lane), qo1 = wqkv_off(j + 8, ntn, lane);
        auto issue = [&]() {
            glds16s<MEGA>(ab, off, sb);
            glds16s<MEGA>(wb, off, sb + 8192);
            if (j < 2) glds16s<MEGA>(wb + 8192, off, sb + 16384);
            ab += 8192; wb += 10240;
            if (sl == NSLOT - 1) { sl = 0; sb -= (NSLOT - 1) * SLOT; } else { ++sl; sb += SLOT; }
        };
#define RS_WAIT(n, x) do { if (j < 2) WAITV(3 * (n) + (x)); else WAITV(2 * (n) + (x)); } while (0)
        if (MEGA) BAR_RAW();
        if (MEGA && wready) {
            glds16s<true>(ab, off, sb); glds16s<true>(ab + 8192, off, sb + SLOT);
            ab += 2 * 8192; wb += 2 * 10240; sl = (SL0 + 2) % NSLOT; sb = sm0 + sl * SLOT + j * 1024;
            issue();
            RS_WAIT(1, 1);
        } else {
            issue(); issue(); issue();
            RS_WAIT(2, 0);
        }
        BAR_RAW();
#pragma unroll 1
        for (int s = 0; s < NS - DEP; ++s) { issue(); RS_WAIT(2, 0); BAR_RAW(); }
        if (MEGA && TAILS) {
#pragma unroll
            for (int s = 0; s < 2; ++s)
#pragma unroll
                for (int p = 0; p < 3; ++p) glds16s<true>(wnext + ntn * 245760 + s * 24576 + p * 8192, off, sm0 + (s + 2) * 40960 + 16384 + p * 8192 + j * 1024);
            RS_WAIT(1, 6); BAR_RAW(); WAITV(6); BAR_RAW();
        } else if (MEGA && wnext && bi < 30) {
#pragma unroll
            for (int s = 0; s < 2; ++s) {
                glds16s<false>(wnext + s * (64 * 3840), qo0, sm0 + QKV_BASE + (s + 2) * 32768 + 16384 + j * 1024);
                glds16s<false>(wnext + s * (64 * 3840), qo1, sm0 + QKV_BASE + (s + 2) * 32768 + 24576 + j * 1024);
            }
            RS_WAIT(1, 4); BAR_RAW(); WAITV(4); BAR_RAW();
        } else { RS_WAIT(1, 0); BAR_RAW(); WAITV(0); BAR_RAW(); }
#undef RS_WAIT
        return;
    }

    const int cw = w - 8, r = lane & 15, g = lane >> 4, row = 16 * cw + r;
    if (resrow) {
#pragma unroll
        for (int j = 0; j < 5; ++j) rv[j] = *(const u32x2*)(res + (unsigned)((tm * 64 + row) * 1280 + tn * 160 + j * 32 + g * 8));
    } else if (!MEGA) {
#pragma unroll
        for (int j = 0; j < 5; ++j) rv[j] = *(const u32x2*)(res + (unsigned)((tm * 80 + tn * 10 + 2 * j + (g >> 1)) * 1024 + row * 16 + (g & 1) * 8));
    }
    const unsigned xa = g * 1024 + row * 16, xt = g * 1024 + row * 8, wa = 8192 + lane * 16;
    f32x4 acc[5];
#pragma unroll
    for (int j = 0; j < 5; ++j) acc[j] = (f32x4){0.f, 0.f, 0.f, 0.f};
    bf16x8 xf[2], wf[2][5];
    if (MEGA) {
        if (w == 8) { if (TAILS) await<2>(ws, tag, cin + (tm >> 2) * 32 + (lane & 31), 2 * (tm & 3)); else await<2>(ws, tag, cin + (tm >> 1) * 16 + (lane & 15), 2 * (tm & 1)); }
        if (!wready) await<4>(ws, tag, CN_R + cw * 64 + lane, 0);
        RT(tp);
        BAR_RAW();
        RT(tg); STAMPV(8, 8, tp); STAMPV(9, 8, tg);
    }
    BAR_RAW();
    STAMPW(1, 8);
    { RT(tb0); STAMPV(11, 8, tb0); }
#pragma unroll
    for (int ks = 0; ks < 2; ++ks) {
        xf[ks] = lds128(smem + SL0 * SLOT + xa + ks * 4096);
#pragma unroll
        for (int j = 0; j < 5; ++j) wf[ks][j] = lds128(smem + SL0 * SLOT + wa + ks * 5120 + j * 1024);
    }
    int slot = SL0;
#define RS_STAGE(TAIL) do { \
        slot = (slot + 1 == NSLOT) ? 0 : slot + 1; \
        LAS char* nb = smem + slot * SLOT; \
        BAR_RAW();                                   \
        SBAR(); \
        _Pragma("unroll") for (int ks = 0; ks < 2; ++ks) { \
            _Pragma("unroll") for (int j = 0; j < 5; ++j) { \
                acc[j] = MFMA16(wf[ks][j], xf[ks], acc[j]); \
                wf[ks][j] = lds128(nb + wa + ks * 5120 + j * 1024); \
                if (j == 4) xf[ks] = (TAIL) ? cat(lds64(nb + xt + ks * 4096), lds64(nb + xt + ks * 4096 + 512)) : lds128(nb + xa + ks * 4096); \
                SBAR(); \
            } } } while (0)
#pragma unroll 1
    for (int s = 0; s < NS - 1 - (TAILS ? 2 : 0); ++s) RS_STAGE(false);
    if (TAILS) {
#pragma unroll 1
        for (int s = 0; s < 2; ++s) RS_STAGE(true);
    }
#undef RS_STAGE
#pragma unroll
    for (int ks = 0; ks < 2; ++ks)
#pragma unroll
        for (int j = 0; j < 5; ++j) acc[j] = MFMA16(wf[ks][j], xf[ks], acc[j]);
    STAMPW(2, 8);
#pragma unroll
    for (int j = 0; j < 5; ++j)
        rv[j] = (u32x2){pack2(acc[j][0] + bflo(rv[j][0]), acc[j][1] + bfhi(rv[j][0])), pack2(acc[j][2] + bflo(rv[j][1]), acc[j][3] + bfhi(rv[j][1]))};
    if (outrow) {
#pragma unroll
        for (int j = 0; j < 5; ++j) st64<false>(out + (unsigned)((tm * 64 + row) * 1280 + tn * 160 + j * 32 + g * 8), rv[j]);
    } else {
        char* op = out + (unsigned)((tm * 80 + tn * 10) * 1024 + row * 16);
#pragma unroll
        for (int jp = 0; jp < 2; ++jp) {
            auto s0 = __builtin_amdgcn_permlane16_swap(rv[2 * jp][0], rv[2 * jp + 1][0], false, false);
            auto s1 = __builtin_amdgcn_permlane16_swap(rv[2 * jp][1], rv[2 * jp + 1][1], false, false);
            st128<MEGA>(op + (4 * jp + 2 * (g & 1) + (g >> 1)) * 1024, (u32x4){s0[0], s1[0], s0[1], s1[1]});
        }
        st64<MEGA>(op + (8 + (g >> 1)) * 1024 + (g & 1) * 8, rv[4]);
        float v[20]; float sum = 0.f;
#pragma unroll
        for (int j = 0; j < 5; ++j) {
            v[4 * j] = bflo(rv[j][0]); v[4 * j + 1] = bfhi(rv[j][0]); v[4 * j + 2] = bflo(rv[j][1]); v[4 * j + 3] = bfhi(rv[j][1]);
            sum += (v[4 * j] + v[4 * j + 1]) + (v[4 * j + 2] + v[4 * j + 3]);
        }
        const float mean = xsum16_32(sum) * (1.f / 80.f);
        float m2 = 0.f;
#pragma unroll
        for (int i = 0; i < 20; ++i) { const float d = v[i] - mean; m2 += d * d; }
        m2 = xsum16_32(m2);
        if (g == 0) st64<MEGA>(stats + (unsigned)((tm * 8 + tn) * 512 + row * 8), (u32x2){__float_as_uint(mean), __float_as_uint(m2)});
        RT(ts);
        if (MEGA) signal(ws, tag, cout + tm * 8 + tn, cw, lane);
        STAMPV(10, 8, ts);
    }
}

constexpr int ACH = 23552, A_V = 12288, VSTEP = 704, VHALF = 352, ANSL = 6, A_Q = ANSL * ACH;
DI float swap_max(float m) { auto rr = __builtin_amdgcn_permlane32_swap(__float_as_uint(m), __float_as_uint(m), false, false); return fmaxf(__uint_as_float(rr[0]), __uint_as_float(rr[1])); }
DI float max16(const f32x16& s) {
    float a = fmaxf(fmaxf(s[0], s[1]), s[2]), b = fmaxf(fmaxf(s[3], s[4]), s[5]);
    a = fmaxf(fmaxf(a, s[6]), s[7]); b = fmaxf(fmaxf(b, s[8]), s[9]);
    a = fmaxf(fmaxf(a, s[10]), s[11]); b = fmaxf(fmaxf(b, s[12]), s[13]);
    a = fmaxf(fmaxf(a, s[14]), s[15]);
    return swap_max(fmaxf(a, b));
}
DI bf16x8 packp(const f32x16& p, int s) {
    u32x4 u = {pack2(p[8 * s], p[8 * s + 1]), pack2(p[8 * s + 2], p[8 * s + 3]), pack2(p[8 * s + 4], p[8 * s + 5]), pack2(p[8 * s + 6], p[8 * s + 7])};
    return __builtin_bit_cast(bf16x8, u);
}

DI void retile_slab(int task, LAS char* scr, const Wts& wt, char* ws, int lane) {
    const unsigned sl = (unsigned)(size_t)scr, l16 = lane * 16;
    const int q = (lane & 15) >> 2, pp = lane & 3;
    if (task < 544) {
        const bool iso = task < 160;
        const int t2 = iso ? task : task - 160, nk = iso ? 20 : 48, ct = t2 / nk, ks = t2 - ct * nk;
#pragma unroll
        for (int p = 0; p < 5; ++p) { const int G = p * 64 + lane, kr = G / 10, c = G - kr * 10, k = ks * 32 + kr;
            const int row = !iso ? k : (k < 512 ? 20 * (k >> 4) + (k & 15) : 20 * ((k - 512) >> 2) + 16 + (k & 3));
            glds16s<false>(iso ? wt.wo : wt.wd, (unsigned)(row * 1280 + ct * 160 + c * 16), sl + p * 1024); }
        WAITV(0);
        LAS const char* rp = scr + (8 * (lane >> 4) + q) * 160 + pp * 8;
#pragma unroll
        for (int i = 0; i < 5; ++i) st128s(ws + (iso ? WS_WO : WS_WD) + t2 * 5120 + i * 1024, l16, __builtin_bit_cast(u32x4, cat(ldstr(rp + i * 32), ldstr(rp + 640 + i * 32))));
    } else if (task < 1184) {
        const int t2 = task - 544, tf = t2 / 40, ks = t2 - tf * 40;
#pragma unroll
        for (int p = 0; p < 3; ++p) { const int G = p * 64 + lane, k = G / 12, c = G - k * 12; const unsigned so = (unsigned)((ks * 16 + k) * 3072 + tf * 192 + c * 16);
            glds16s<false>(wt.wg, so, sl + p * 1024); glds16s<false>(wt.wu, so, sl + 3072 + p * 1024); }
        WAITV(0);
        LAS const char* rp = scr + ((lane >> 4) & 1) * 3072 + (8 * (lane >> 5) + q) * 192 + pp * 8;
#pragma unroll
        for (int i = 0; i < 6; ++i) st128s(ws + WS_WGU + t2 * 6144 + i * 1024, l16, __builtin_bit_cast(u32x4, cat(ldstr(rp + i * 32), ldstr(rp + 768 + i * 32))));
    }
}

template <bool MEGA>
DI void phase_attn(LAS char* smem, const char* QK, const char* V, char* O, char* ws, const Wts& wt, bool first, int cin, int cout, unsigned tag, int wave DBG_ARG) {
    PHASE_IDS();
    const int head = 16 * e + (bi >> 1), qt = 2 * pr + (bi & 1), par = head & 1;
    const unsigned sm0 = (unsigned)(size_t)smem, l16 = lane * 16;
    const int g0 = 5 * (head >> 1) + 3 * par, gt = 5 * (head >> 1) + 2;
    auto kq_src = [&](int sec, int blk256, int i) { const int grp = i / 3, part = i - 3 * grp; return QK + ((blk256 * 4 + grp) * 160 + sec * 80 + (part == 2 ? gt : g0 + part)) * 1024; };
    auto v_off = [&](int i, bool& ok) { const int G = i * 64 + lane, sh = G / 22, row = G - sh * 22; ok = row < 20; return (unsigned)((sh * 640 + head * 20 + row) * 16); };
    const int c0 = 2 * pr;
    if (MEGA) {
        if (w == 8) { const int sec = min(lane >> 3, 2), ct = (head * 20 + ((lane >> 2) & 1) * 19) >> 7;
                      await<4>(ws, tag, cin + (5 * sec + ct) * 16 + (sec ? 2 * c0 + (lane & 3) : 2 * qt + (lane & 1)), 0); }
        RT(tp);
        BAR_RAW();
        RT(tg); STAMPV(8, 8, tp); STAMPV(9, 8, tg);
    }
    {
        bool ok; const unsigned vo = v_off(w, ok);
        glds16s<MEGA>(kq_src(1, c0, w), l16, sm0 + w * 1024);
        glds16s<MEGA>(kq_src(0, qt, w), l16, sm0 + A_Q + w * 1024);
        if (w < 11 && ok) glds16s<MEGA>(V + c0 * 327680, vo, sm0 + A_V + w * 1024);
        if (tid < ANSL * 64) {
            const unsigned cv = (tid & 1) ? 0u : 0x3F803F80u;
            *(LAS u32x4*)(smem + (tid >> 6) * ACH + A_V + ((tid & 63) >> 1) * VHALF + 320 + (tid & 1) * 16) = (u32x4){cv, cv, cv, cv};
        }
    }
    if (w >= 8) {
        const int lw = w - 8;
        __builtin_amdgcn_s_setprio(3);
        unsigned vo[3]; bool ok[3];
#pragma unroll
        for (int i = 0; i < 3; ++i) vo[i] = v_off(lw + 4 * i, ok[i]);
        auto chunk = [&](int c) {
            const int cc = (c + c0) & 7; const unsigned sb = sm0 + (c >= ANSL ? c - ANSL : c) * ACH;
#pragma unroll
            for (int i = 0; i < 3; ++i) glds16s<MEGA>(kq_src(1, cc, lw + 4 * i), l16, sb + (lw + 4 * i) * 1024);
#pragma unroll
            for (int i = 0; i < 3; ++i) if ((i < 2 || lw < 3) && ok[i]) glds16s<MEGA>(V + cc * 327680, vo[i], sb + A_V + (lw + 4 * i) * 1024);
        };
        asm volatile("s_waitcnt vmcnt(0) lgkmcnt(0)\n\ts_barrier" ::: "memory");
        chunk(1);
        if (MEGA) await<4>(ws, tag, cin + (5 + (lane >> 5) * 5 + ((head * 20 + ((lane >> 4) & 1) * 19) >> 7)) * 16 + (lane & 15), 0);
        chunk(2);
        WAITV(0); BAR_RAW();
        chunk(3); chunk(4); chunk(5);
        WAITV(0); BAR_RAW();
        chunk(6); chunk(7);
        if (first) {
            if (lw < 3) retile_slab(bid + 256 * lw, smem + A_Q + lw * 6144, wt, ws, lane);
            WAITV(0); BAR_RAW();
            if (lw >= 2) retile_slab(bid + 256 * (lw + 1), smem + 2 * ACH + lw * 6144, wt, ws, lane);
            if (MEGA) signal(ws, tag, CN_R + bid, lw, lane);
        } else {
            WAITV(0);
            const int t = tid - 512;
            unsigned tv = *(const unsigned*)(ws + WS_WO + e * 409600 + bi * 12800 + min(t, 99) * 128);
            tv ^= *(const unsigned*)(ws + WS_WGU + e * 1966080 + bi * 61440 + t * 128);
            tv ^= *(const unsigned*)(ws + WS_WGU + e * 1966080 + bi * 61440 + min(256 + t, 479) * 128);
            BAR_RAW();
            asm volatile("" ::"v"(tv));
            if (MEGA) { prefetch_res<4>(sm0, ws + WS_WO + (4 * e + (bi >> 3)) * 102400, 3, lw, l16); WAITV(0); }
        }
        __builtin_amdgcn_s_setprio(0);
        if (MEGA) BAR_RAW();
        return;
    }

    const int r = lane & 31, h = lane >> 5;
    const unsigned k0a = h * 1024 + r * 16, k1a = 2048 + r * 16 + par * 8, va = A_V + h * VHALF + min(r, 21) * 16;
    f32x16 zero, negm, o;
#pragma unroll
    for (int i = 0; i < 16; ++i) { zero[i] = 0.f; o[i] = 0.f; }
    bf16x8 v1 = {0, 0, 0, 0, 0, 0, 0, 0};
    u32x4 pw0 = {0, 0, 0, 0}, pw1 = {0, 0, 0, 0};
    float e0 = 0.f, e1 = 0.f, e2 = 0.f, e3 = 0.f;
    PIN(o); PIN(v1); PIN(pw0); PIN(pw1);
    asm volatile("s_waitcnt vmcnt(0) lgkmcnt(0)\n\ts_barrier" ::: "memory");
    STAMPW(1, 0);
    { RT(tb0); STAMPV(11, 0, tb0); }
    bf16x8 q0; bf16x4 q1;
    { LAS const char* qp = smem + A_Q + (w >> 1) * 3072 + (w & 1) * 512;
      q0 = lds128(qp + k0a); q1 = lds64(qp + k1a);
      if (h) q1 = (bf16x4){0, 0, 0, 0}; }
    LAS const char* pk0 = smem + k0a;
    LAS const char* pk1 = smem + k1a;
    LAS const char* pv = smem + va;
    f32x16 sA, sB;
    {
        const bf16x8 a = lds128(pk0); const bf16x4 b = lds64(pk1);
        f32x16 s = MFMA32(a, q0, zero);
        s = MFMA32K8(b, q1, s);
        const float m = max16(s);
#pragma unroll
        for (int i = 0; i < 16; ++i) negm[i] = -m;
        sA = MFMA32(a, q0, negm);
        sA = MFMA32K8(b, q1, sA);
    }
    bf16x8 ka = lds128(pk0 + 512), v0 = lds128(pv);
    bf16x4 kb = lds64(pk1 + 512);
#define EX(x) __builtin_amdgcn_exp2f(x)
#define ATT_STEP(SN, SC, PK0, PK1, PV0, PV1) do { \
        float t0, t1, t2, t3; \
        SN = MFMA32(ka, q0, negm); PIN(SN); ka = lds128(PK0); \
        pw1[2] = pack2(e0, e1); pw1[3] = pack2(e2, e3); PIN(pw1); SBAR(); \
        t0 = EX(SC[0]); t1 = EX(SC[1]); t2 = EX(SC[2]); t3 = EX(SC[3]); PIN(t0); PIN(t1); PIN(t2); PIN(t3); SBAR(); \
        o = MFMA32(v1, __builtin_bit_cast(bf16x8, pw1), o); PIN(o); v1 = lds128(PV1); \
        pw0[0] = pack2(t0, t1); pw0[1] = pack2(t2, t3); PIN(pw0); SBAR(); \
        t0 = EX(SC[4]); t1 = EX(SC[5]); t2 = EX(SC[6]); t3 = EX(SC[7]); PIN(t0); PIN(t1); PIN(t2); PIN(t3); SBAR(); \
        SN = MFMA32K8(kb, q1, SN); PIN(SN); kb = lds64(PK1); \
        pw0[2] = pack2(t0, t1); pw0[3] = pack2(t2, t3); PIN(pw0); SBAR(); \
        t0 = EX(SC[8]); t1 = EX(SC[9]); t2 = EX(SC[10]); t3 = EX(SC[11]); PIN(t0); PIN(t1); PIN(t2); PIN(t3); SBAR(); \
        o = MFMA32(v0, __builtin_bit_cast(bf16x8, pw0), o); PIN(o); v0 = lds128(PV0); \
        pw1[0] = pack2(t0, t1); pw1[1] = pack2(t2, t3); PIN(pw1); SBAR(); \
        e0 = EX(SC[12]); e1 = EX(SC[13]); e2 = EX(SC[14]); e3 = EX(SC[15]); PIN(e0); PIN(e1); PIN(e2); PIN(e3); SBAR(); \
    } while (0)
#define KOF(u) (((u) >> 1) * 3072 + ((u) & 1) * 512)
#pragma unroll 1
    for (int c = 0; c < 8; ++c) {
        const int step = (c == ANSL - 1) ? -(ANSL - 1) * ACH : ACH;
        LAS const char* nk0 = pk0 + step; LAS const char* nk1 = pk1 + step; LAS const char* nv = pv + step;
        if (c == 4) __builtin_amdgcn_s_setprio(2); else if (c == 0 || c == 2 || c == 5 || c == 7) __builtin_amdgcn_s_setprio(1);
        ATT_STEP(sB, sA, pk0 + KOF(2), pk1 + KOF(2), pv + 2 * VSTEP, pv + 1 * VSTEP);
        ATT_STEP(sA, sB, pk0 + KOF(3), pk1 + KOF(3), pv + 4 * VSTEP, pv + 3 * VSTEP);
        ATT_STEP(sB, sA, pk0 + KOF(4), pk1 + KOF(4), pv + 6 * VSTEP, pv + 5 * VSTEP);
        ATT_STEP(sA, sB, pk0 + KOF(5), pk1 + KOF(5), pv + 8 * VSTEP, pv + 7 * VSTEP);
        if (c == 1 || c == 6) __builtin_amdgcn_s_setprio(2); else if (c != 3 && c != 4) __builtin_amdgcn_s_setprio(0);
        ATT_STEP(sB, sA, pk0 + KOF(6), pk1 + KOF(6), pv + 10 * VSTEP, pv + 9 * VSTEP);
        ATT_STEP(sA, sB, pk0 + KOF(7), pk1 + KOF(7), pv + 12 * VSTEP, pv + 11 * VSTEP);
        if (c == 0 || c == 2 || c == 5) { BAR_RAW(); __builtin_amdgcn_s_setprio(3); }
        ATT_STEP(sB, sA, nk0, nk1, pv + 14 * VSTEP, pv + 13 * VSTEP);
        ATT_STEP(sA, sB, nk0 + KOF(1), nk1 + KOF(1), nv, pv + 15 * VSTEP);
        pk0 = nk0; pk1 = nk1; pv = nv;
    }
#undef KOF
#undef ATT_STEP
    pw1[2] = pack2(e0, e1); pw1[3] = pack2(e2, e3);
    o = MFMA32(v1, __builtin_bit_cast(bf16x8, pw1), o);
    STAMPW(2, 0);
    float l;
    { auto rr = __builtin_amdgcn_permlane32_swap(__float_as_uint(o[8]), __float_as_uint(o[8]), false, false); l = __uint_as_float(rr[1]); }
    float chk = 0.f;
#pragma unroll
    for (int i = 0; i < 12; ++i) chk += o[i] * 0.f;
    const bool okr = (chk == 0.f) && (l <= 1.8446744e19f);
    if (__builtin_expect(!__all(okr), 0)) {
        float m = -INFINITY;
#pragma unroll
        for (int i = 0; i < 16; ++i) o[i] = 0.f;
        const unsigned cv = r == 20 ? 0x3F803F80u : 0u;
#pragma unroll 1
        for (int kbk = 0; kbk < 64; ++kbk) {
            const char* kp = QK + ((kbk >> 1) * 160 + 80) * 1024 + ((kbk & 1) * 32 + r) * 16;
            const u32x2 ka0 = ld64<MEGA>(kp + (g0 + h) * 1024), ka1 = ld64<MEGA>(kp + (g0 + h) * 1024 + 8);
            f32x16 s = MFMA32(__builtin_bit_cast(bf16x8, (u32x4){ka0[0], ka0[1], ka1[0], ka1[1]}), q0, zero);
            s = MFMA32K8(__builtin_bit_cast(bf16x4, ld64<MEGA>(kp + gt * 1024 + par * 8)), q1, s);
            const float mn = fmaxf(m, max16(s));
            const float al = __builtin_amdgcn_exp2f(m - mn);
            m = mn;
#pragma unroll
            for (int i = 0; i < 16; ++i) { o[i] *= al; s[i] = __builtin_amdgcn_exp2f(s[i] - mn); }
#pragma unroll
            for (int st = 0; st < 2; ++st) {
                const char* vq = V + (((2 * kbk + st) * 2 + h) * 640 + head * 20 + min(r, 19)) * 16;
                const u32x2 va0 = ld64<MEGA>(vq), va1 = ld64<MEGA>(vq + 8);
                u32x4 vv = {va0[0], va0[1], va1[0], va1[1]};
                if (r >= 20) vv = (u32x4){cv, cv, cv, cv};
                o = MFMA32(__builtin_bit_cast(bf16x8, vv), packp(s, st), o);
            }
        }
        auto rr = __builtin_amdgcn_permlane32_swap(__float_as_uint(o[8]), __float_as_uint(o[8]), false, false); l = __uint_as_float(rr[1]);
    }
    {
        const float inv = 1.f / l;
        char* ob = O + (unsigned)((qt * 4 + (w >> 1)) * 81920);
        const int row = (w & 1) * 32 + r;
        auto s0 = __builtin_amdgcn_permlane32_swap(pack2(o[0] * inv, o[1] * inv), pack2(o[4] * inv, o[5] * inv), false, false);
        auto s1 = __builtin_amdgcn_permlane32_swap(pack2(o[2] * inv, o[3] * inv), pack2(o[6] * inv, o[7] * inv), false, false);
        st128<MEGA>(ob + (unsigned)((2 * head + h) * 1024 + row * 16), (u32x4){s0[0], s1[0], s0[1], s1[1]});
        if (h == 0) st64<MEGA>(ob + (unsigned)(65536 + head * 512 + row * 8), (u32x2){pack2(o[8] * inv, o[9] * inv), pack2(o[10] * inv, o[11] * inv)});
    }
    RT(ts);
    if (MEGA) { signal(ws, tag, cout + qt * 32 + head, w, lane); RT(td); STAMPV(12, 7, td); BAR_RAW(); }
    STAMPV(10, 0, ts); STAMPV(13, 7, ts);
#undef EX
}

#define SMEM_DECL(n) __shared__ __attribute__((aligned(1024))) char smem_[n]; LAS char* smem = (LAS char*)smem_
#define PH(k, wv, call) call
template <bool MEGA>
DI void run_phase(int ph, int layer, LAS char* smem, const char* x0, const char* wqkv, const Wts& wt, char* ws, char* out, u32x2 (&rv)[5], unsigned& tag, int wave) {
    if (ph == 0) PH(1, 8, (phase_ln<0, true, MEGA>(smem, x0, wqkv, nullptr, ws + WS_QK, ws + WS_V, ws, wt, 0, CN_Q, tag, wave DBG_PASS(1))));
    if (ph == 1) PH(6, 8, (phase_ln<0, false, MEGA>(smem + QKV_BASE, ws + WS_XB, wqkv, ws + WS_ST + SZ_ST, ws + WS_QK + 2 * SZ_X, ws + WS_V + SZ_X, ws, wt, CN_X + 256, CN_Q + 256, tag, wave DBG_PASS(6))));
    if (ph == 2) PH(2 + 5 * layer, 0, (phase_attn<MEGA>(smem, ws + WS_QK + layer * 2 * SZ_X, ws + WS_V + layer * SZ_X, ws + WS_O + layer * SZ_X, ws, wt, layer == 0,
                                       CN_Q + 256 * layer, CN_O + 256 * layer, tag, wave DBG_PASS(2 + 5 * layer))));
    if (ph == 3) PH(3 + 5 * layer, 8, (phase_res<DM, MEGA>(smem, ws + WS_O + layer * SZ_X, ws + WS_WO, layer ? ws + WS_XB : x0, ws + WS_XA + layer * SZ_X,
                                       ws + WS_ST + 2 * layer * SZ_ST, ws, layer == 0, false, rv, CN_O + 256 * layer, CN_X + 512 * layer, layer != 0, ws + WS_WGU, tag, wave DBG_PASS(3 + 5 * layer))));
    if (ph == 4) PH(4 + 5 * layer, 8, (phase_ln<1, false, MEGA>(smem, ws + WS_XA + layer * SZ_X, ws + WS_WGU, ws + WS_ST + 2 * layer * SZ_ST,
                                       ws + WS_H + layer * (SEQ * FF * 2), nullptr, ws, wt, CN_X + 512 * layer, CN_H + 256 * layer, tag, wave DBG_PASS(4 + 5 * layer))));
    if (ph == 5) PH(5 + 5 * layer, 8, (phase_res<FF, MEGA>(smem, ws + WS_H + layer * (SEQ * FF * 2), ws + WS_WD, ws + WS_XA + layer * SZ_X, layer ? out : ws + WS_XB,
                                       ws + WS_ST + SZ_ST, ws, false, layer != 0, rv, CN_H + 256 * layer, CN_X + 256, true, layer ? nullptr : wqkv, tag, wave DBG_PASS(5 + 5 * layer))));
}
__global__ __launch_bounds__(768) void k_mega(const char* x0, const char* wqkv, const char* wo, const char* wg, const char* wu, const char* wd,
                                              char* ws, char* out) {
    SMEM_DECL(LDS_BYTES);
    const Wts wt = {wo, wg, wu, wd};
    u32x2 rv[5];
#pragma unroll
    for (int j = 0; j < 5; ++j) rv[j] = (u32x2){0u, 0u};
    unsigned tag = 0;
    const int wave = __builtin_amdgcn_readfirstlane(threadIdx.x >> 6);
    run_phase<true>(0, 0, smem, x0, wqkv, wt, ws, out, rv, tag, wave);
#pragma unroll 1
    for (int layer = 0; layer < 2; ++layer) {
        if (layer) run_phase<true>(1, layer, smem, x0, wqkv, wt, ws, out, rv, tag, wave);
        run_phase<true>(2, layer, smem, x0, wqkv, wt, ws, out, rv, tag, wave);
        run_phase<true>(3, layer, smem, x0, wqkv, wt, ws, out, rv, tag, wave);
        run_phase<true>(4, layer, smem, x0, wqkv, wt, ws, out, rv, tag, wave);
        run_phase<true>(5, layer, smem, x0, wqkv, wt, ws, out, rv, tag, wave);
    }
    if (blockIdx.x == 0 && wave == 8) __hip_atomic_store((GAS unsigned*)(ws + WS_EPOCH), tag, __ATOMIC_RELAXED, __HIP_MEMORY_SCOPE_AGENT);
}

extern "C" void kernel_launch(void* const* d_in, const int* in_sizes, int n_in, void* d_out, int out_size, void* d_ws, size_t ws_size,
                              hipStream_t stream) {
    const char* x0 = (const char*)d_in[0];
    const char* wqkv = (const char*)d_in[1];
    const char* wo = (const char*)d_in[2];
    const char* wg = (const char*)d_in[4];
    const char* wu = (const char*)d_in[5];
    const char* wd = (const char*)d_in[6];
    char* ws = (char*)d_ws;
    char* out = (char*)d_out;
    k_mega<<<256, 768, 0, stream>>>(x0, wqkv, wo, wg, wu, wd, ws, out);
}
```

```cpp
#include <hip/hip_runtime.h>
#include <hip/hip_bf16.h>

typedef short bf16x8 __attribute__((ext_vector_type(8)));
typedef short bf16x4 __attribute__((ext_vector_type(4)));
typedef float f32x16 __attribute__((ext_vector_type(16)));
typedef float f32x4 __attribute__((ext_vector_type(4)));
typedef float f32x2 __attribute__((ext_vector_type(2)));
typedef unsigned u32x4 __attribute__((ext_vector_type(4)));
typedef unsigned u32x2 __attribute__((ext_vector_type(2)));
typedef __bf16 bf2_t __attribute__((ext_vector_type(2)));
#define LAS __attribute__((address_space(3)))
#define GAS __attribute__((address_space(1)))
#define DI __device__ __forceinline__
#define MFMA32(a, b, c) __builtin_amdgcn_mfma_f32_32x32x16_bf16((a), (b), (c), 0, 0, 0)
#define MFMA32K8(a, b, c) __builtin_amdgcn_mfma_f32_32x32x8bf16_1k((a), (b), (c), 0, 0, 0)
#define MFMA4(a, b, c) __builtin_amdgcn_mfma_f32_4x4x4bf16_1k((a), (b), (c), 0, 0, 0)
#define MFMA16(a, b, c) __builtin_amdgcn_mfma_f32_16x16x32_bf16((a), (b), (c), 0, 0, 0)
#define WAITV(n) asm volatile("s_waitcnt vmcnt(%0)" ::"i"(n) : "memory")
#define BAR_LGKM() asm volatile("s_waitcnt lgkmcnt(0)\n\ts_barrier" ::: "memory")
#define BAR_RAW() asm volatile("s_barrier" ::: "memory")
#define PIN(x) asm volatile("" : "+v"(x))
#define SBAR() __builtin_amdgcn_sched_barrier(0)

constexpr int SEQ = 2048, DM = 640, NH = 32, DH = 20, FF = 1536;
constexpr float QSCALE = 0.22360679774997896f * 1.4426950408889634f;
constexpr float LN_EPS = 1e-5f;
constexpr int LDS_BYTES = 163840;
constexpr int CODE_LINES = 392;

constexpr unsigned SZ_X = SEQ * DM * 2, SZ_ST = 32 * 8 * 512;
constexpr unsigned WS_XA = 0;
constexpr unsigned WS_XB = WS_XA + 2 * SZ_X;
constexpr unsigned WS_O = WS_XB + SZ_X;
constexpr unsigned WS_V = WS_O + 2 * SZ_X;
constexpr unsigned WS_QK = WS_V + 2 * SZ_X;
constexpr unsigned WS_H = WS_QK + 4 * SZ_X;
constexpr unsigned WS_ST = WS_H + 2 * SEQ * FF * 2;
constexpr unsigned WS_WO = WS_ST + 3 * SZ_ST;
constexpr unsigned WS_WD = WS_WO + DM * DM * 2;
constexpr unsigned WS_WGU = WS_WD + FF * DM * 2;
constexpr unsigned WS_CSQ = WS_WGU + 2 * DM * FF * 2;
constexpr unsigned WS_CSGU = WS_CSQ + 131072;
constexpr unsigned WS_EPOCH = WS_CSGU + 16384, WS_CNT = WS_EPOCH + 128;
constexpr int CN_O = 0, CN_X = CN_O + 512, CN_H = CN_X + 768, CN_Q = CN_H + 512, CN_R = CN_Q + 512;

#define STAMPV(i, wv, val)
#define RT(x)
#define DBG_ARG
#define DBG_PASS(k)
#define STAMPW(i, wv)

#define EXP_ROT
#define NOSTAT 0
#define NOSLAB 0
#define PHASE_IDS() \
    int lane, w = wave, bid = __builtin_amdgcn_readfirstlane(blockIdx.x); \
    asm volatile("v_mbcnt_lo_u32_b32 %0, -1, 0\n\tv_mbcnt_hi_u32_b32 %0, -1, %0" : "=v"(lane)); asm volatile("" : "+s"(bid), "+s"(w)); \
    const int tid = w * 64 + lane, xcd = bid & 7, bi = bid >> 3, pr = xcd >> 1, e = xcd & 1
DI unsigned pack2(float a, float b) { f32x2 v = {a, b}; return __builtin_bit_cast(unsigned, __builtin_convertvector(v, bf2_t)); }
DI float bflo(unsigned u) { return __uint_as_float(u << 16); }
DI float bfhi(unsigned u) { return __uint_as_float(u & 0xffff0000u); }
DI unsigned long long sgpr64(const void* p) {
    return ((unsigned long long)(unsigned)__builtin_amdgcn_readfirstlane((int)((unsigned long long)p >> 32)) << 32) |
           (unsigned)__builtin_amdgcn_readfirstlane((int)(unsigned long long)p);
}
template <bool DEV>
DI void glds16s(const char* sb_, unsigned voff, unsigned ldsaddr) {
    const unsigned long long sbase = sgpr64(sb_);
    ldsaddr = __builtin_amdgcn_readfirstlane(ldsaddr);
    if (DEV) asm volatile("s_mov_b32 m0, %2\n\ts_nop 3\n\tglobal_load_lds_dwordx4 %1, %0 sc1" ::"s"(sbase), "v"(voff), "s"(ldsaddr) : "memory");
    else asm volatile("s_mov_b32 m0, %2\n\ts_nop 3\n\tglobal_load_lds_dwordx4 %1, %0" ::"s"(sbase), "v"(voff), "s"(ldsaddr) : "memory");
}
template <bool DEV> DI void st128(char* p, u32x4 v) {
    if (DEV) asm volatile("global_store_dwordx4 %0, %1, off sc1\n\ts_nop 1" ::"v"(p), "v"(v) : "memory"); else *(u32x4*)p = v;
}
template <bool DEV> DI void st64(char* p, u32x2 v) {
    if (DEV) asm volatile("global_store_dwordx2 %0, %1, off sc1" ::"v"(p), "v"(v) : "memory"); else *(u32x2*)p = v;
}
template <bool DEV> DI u32x2 ld64(const char* p) {
    if (DEV) return __builtin_bit_cast(u32x2, __hip_atomic_load((const GAS unsigned long long*)p, __ATOMIC_RELAXED, __HIP_MEMORY_SCOPE_AGENT));
    return *(const u32x2*)p;
}
DI void st128s(char* sb_, unsigned voff, u32x4 v) {
    asm volatile("s_nop 4\n\tglobal_store_dwordx4 %1, %2, %0 sc1\n\ts_nop 1" ::"s"(sgpr64(sb_)), "v"(voff), "v"(v) : "memory");
}
DI unsigned get_tag(char* ws) {
    return (unsigned)__builtin_amdgcn_readfirstlane((int)__hip_atomic_load((const GAS unsigned*)(ws + WS_EPOCH), __ATOMIC_RELAXED, __HIP_MEMORY_SCOPE_AGENT)) + 1u;
}
template <bool DEV> DI void st32(char* p, float v) {
    if (DEV) __hip_atomic_store((GAS float*)p, v, __ATOMIC_RELAXED, __HIP_MEMORY_SCOPE_AGENT); else *(float*)p = v;
}
template <bool DEV> DI float ld32(const char* p) {
    if (DEV) return __hip_atomic_load((const GAS float*)p, __ATOMIC_RELAXED, __HIP_MEMORY_SCOPE_AGENT);
    return *(const float*)p;
}
DI void signal(char* ws, unsigned tag, int line, int word, int lane) {
    WAITV(0);
    asm volatile("" : "+s"(tag));
    if (lane == 0) __hip_atomic_store((GAS unsigned*)(ws + WS_CNT + line * 128 + word * 4), tag, __ATOMIC_RELAXED, __HIP_MEMORY_SCOPE_AGENT);
}
template <int NWD>
DI void await(char* ws, unsigned tag, int line, int word) {
    const char* p = ws + WS_CNT + line * 128 + word * 4;
    for (unsigned spins = 0; spins < (1u << 20); ++spins) {
        bool ok;
        if (NWD == 2) { u32x2 v; asm volatile("global_load_dwordx2 %0, %1, off sc1\n\ts_waitcnt vmcnt(0)" : "=&v"(v) : "v"(p) : "memory"); ok = v[0] == tag && v[1] == tag; }
        else { u32x4 v; asm volatile("global_load_dwordx4 %0, %1, off sc1\n\ts_waitcnt vmcnt(0)" : "=&v"(v) : "v"(p) : "memory"); ok = v[0] == tag && v[1] == tag && v[2] == tag && v[3] == tag; }
        if (__all(ok)) break;
        __builtin_amdgcn_s_sleep(1);
    }
}
DI bf16x8 lds128(LAS const char* p) { return *(LAS const bf16x8*)p; }
DI bf16x4 lds64(LAS const char* p) { return *(LAS const bf16x4*)p; }
DI bf16x4 ldstr(LAS const char* p) { return __builtin_bit_cast(bf16x4, __builtin_amdgcn_ds_read_tr16_b64_v4i16((LAS bf16x4*)p)); }
DI bf16x8 cat(bf16x4 a, bf16x4 b) { return __builtin_shufflevector(a, b, 0, 1, 2, 3, 4, 5, 6, 7); }
DI float quad_sum(float v) {
    v += __int_as_float(__builtin_amdgcn_mov_dpp(__float_as_int(v), 0xB1, 0xf, 0xf, true));
    return v + __int_as_float(__builtin_amdgcn_mov_dpp(__float_as_int(v), 0x4E, 0xf, 0xf, true));
}
DI float half_sum(float v) { auto a = __builtin_amdgcn_permlane32_swap(__float_as_uint(v), __float_as_uint(v), false, false); return __uint_as_float(a[0]) + __uint_as_float(a[1]); }
DI float xsum16_32(float v) {
    auto a = __builtin_amdgcn_permlane16_swap(__float_as_uint(v), __float_as_uint(v), false, false);
    return half_sum(__uint_as_float(a[0]) + __uint_as_float(a[1]));
}
DI u32x4 split3(float v, bool rowside) {
    const unsigned p1 = pack2(v, v); const float r1 = v - bflo(p1);
    const unsigned p2 = pack2(r1, r1); const float r2 = r1 - bflo(p2);
    const unsigned p3 = pack2(r2, r2);
    const unsigned l1 = p1 & 0xffffu, l2 = p2 & 0xffffu, h1 = p1 & 0xffff0000u, h2 = p2 & 0xffff0000u, h3 = p3 & 0xffff0000u;
    return rowside ? (u32x4){p1, l2 | h1, l2 | h3, 0u} : (u32x4){l1 | h2, l1 | h3, l2 | h1, 0u};
}
DI bf16x4 lo4(bf16x8 v) { return __builtin_shufflevector(v, v, 0, 1, 2, 3); }
DI bf16x4 hi4(bf16x8 v) { return __builtin_shufflevector(v, v, 4, 5, 6, 7); }

template <int MODE> struct LnCfg;
template <> struct LnCfg<0> { static constexpr int SLOT = 32768, NB = 2, PPW = 4, NCS = 128; };
template <> struct LnCfg<1> { static constexpr int SLOT = 40960, NB = 3, PPW = 5, NCS = 192; };
constexpr int T_XC = 0, T_WC = 2048, T_RS = 5120, T_RAW = 5632;

template <int MODE, bool FIRST, bool SWAP, bool MEGA>
DI void ln_mainloop(LAS char* smem, f32x16 (&acc)[LnCfg<MODE>::NB][2], int wm, int wn, int lane, char* ws, int tm, int bid, int cin, unsigned tag, unsigned long long& tb0 DBG_ARG) {
    using C = LnCfg<MODE>;
    constexpr int NB = C::NB;
    const int r = lane & 31, h = lane >> 5, q = (lane & 15) >> 2, pp = lane & 3, blk = (lane >> 4) & 1;
    unsigned xa[4], wa[2];
    if (FIRST) {
        const int row = 64 * wm + 32 * wn + r, sw = (row >> 1) & 7;
#pragma unroll
        for (int ks = 0; ks < 4; ++ks) xa[ks] = row * 128 + (((2 * ks + h) ^ sw) << 4);
    } else xa[0] = wm * 8192 + h * 1024 + r * 16;
    if (MODE == 0) {
#pragma unroll
        for (int cb = 0; cb < 2; ++cb) wa[cb] = 16384 + (8 * h + q) * 256 + (((2 * wn + (FIRST ? cb ^ wm : cb)) ^ q) * 64) + blk * 32 + pp * 8;
    } else { wa[0] = 16384 + wn * 3072 + lane * 16; wa[1] = 0; }
    bf16x8 xf[2][2], wf[2][NB];
    f32x4 sx = {0.f, 0.f, 0.f, 0.f}, sw_ = sx; f32x16 sxx;
#pragma unroll
    for (int i = 0; i < 16; ++i) sxx[i] = 0.f;
    const short sv = ((lane & 15) == ((lane >> 4) & 1)) ? (short)0x3F80 : (short)0;
    const bf16x8 sel = {sv, sv, sv, sv, sv, sv, sv, sv};
#define LN_READ(BUF, SB, KS) do { \
        _Pragma("unroll") for (int tb = 0; tb < 2; ++tb) xf[BUF][tb] = FIRST ? lds128((SB) + (xa[KS] ^ (tb * 4096))) : lds128((SB) + xa[0] + (KS) * 2048 + tb * 512); \
        _Pragma("unroll") for (int j = 0; j < NB; ++j) { \
            if (MODE == 0) wf[BUF][j] = cat(ldstr((SB) + wa[j] + (KS) * 4096), ldstr((SB) + wa[j] + (KS) * 4096 + 1024)); \
            else wf[BUF][j] = lds128((SB) + wa[0] + (KS) * 6144 + j * 1024); } \
    } while (0)
#define LN_MMA(BUF) do { \
        _Pragma("unroll") for (int j = 0; j < NB; ++j) _Pragma("unroll") for (int tb = 0; tb < 2; ++tb) \
            acc[j][tb] = SWAP ? MFMA32(wf[BUF][j], xf[BUF][tb], acc[j][tb]) : MFMA32(xf[BUF][tb], wf[BUF][j], acc[j][tb]); \
        if (FIRST && !NOSTAT) { sx = MFMA16(sel, xf[BUF][0], sx); sw_ = MFMA16(sel, wf[BUF][0], sw_); sxx = MFMA32(xf[BUF][0], xf[BUF][0], sxx); } \
    } while (0)
#define LN_STEP(RB, SB, KS, MB) do { LN_READ(RB, SB, KS); LN_MMA(MB); \
        _Pragma("unroll") for (int i_ = 0; i_ < 3; ++i_) { __builtin_amdgcn_sched_group_barrier(0x8, 1, 0); __builtin_amdgcn_sched_group_barrier(0x100, 2, 0); } \
        __builtin_amdgcn_sched_group_barrier(0x8, 2 * NB - 3 + (FIRST ? 3 : 0), 0); \
        SBAR(); } while (0)
    if (MEGA && !FIRST) {
#pragma unroll
        for (int j = 0; j < NB; ++j) { PIN(acc[j][0]); PIN(acc[j][1]); }
        PIN(xa[0]); PIN(wa[0]); if (MODE == 0) PIN(wa[1]);
        if (wm + wn == 0) await<4>(ws, tag, cin + 16 * tm + (lane & 15), 0);
        RT(tp);
        BAR_RAW();
        RT(tg); STAMPV(8, 8, tp); STAMPV(9, 8, tg);
    }
    BAR_RAW();
    LN_READ(0, smem + 2 * C::SLOT, 0);
#pragma unroll 1
    for (int s = 0; s < 9; ++s) {
        LAS char* sb = smem + ((s + 2) & 3) * C::SLOT;
        LAS char* nb = smem + ((s + 3) & 3) * C::SLOT;
        SBAR();
        LN_STEP(1, sb, 1, 0);
        LN_STEP(0, sb, 2, 1);
        LN_STEP(1, sb, 3, 0);
        BAR_RAW();
        SBAR();
        LN_STEP(0, nb, 0, 1);
    }
    LAS char* tab = smem + C::SLOT;
    {
        LAS char* sb = smem + 3 * C::SLOT;
        SBAR();
        LN_STEP(1, sb, 1, 0);
        LN_STEP(0, sb, 2, 1);
        LN_STEP(1, sb, 3, 0);
        if (!FIRST) BAR_LGKM();
        LN_MMA(1);
    }
    if (FIRST) {
        LAS float* raw = (LAS float*)(tab + T_RAW) + (wn * 2 + wm) * 96;
        if (lane < 16) { raw[lane] = sx[0]; raw[16 + lane] = sx[1]; raw[32 + lane] = sw_[0]; raw[48 + lane] = sw_[1]; }
        { const int ri = (r & 3) + 4 * (r >> 3); float dg = sxx[0];
#pragma unroll
          for (int i = 1; i < 16; ++i) dg = ri == i ? sxx[i] : dg;
          if (((r >> 2) & 1) == h) raw[64 + r] = dg; }
        asm volatile("s_waitcnt lgkmcnt(0)" ::: "memory");
        const float csum = raw[32 + r];
        const float mu = raw[r] * (1.f / DM), var = fmaxf(raw[64 + r] * (1.f / DM) - mu * mu, 0.f);
        const int tl = 64 * wm + 32 * wn + r, nl = 64 * wn + 32 * wm + r;
        if (h == 0) { *(LAS u32x4*)(tab + T_XC + tl * 16) = split3(-mu, true); *(LAS float*)(tab + T_RS + tl * 4) = __builtin_amdgcn_rsqf(var + LN_EPS); }
        else { *(LAS u32x4*)(tab + T_WC + nl * 16) = split3(csum, false); st32<MEGA>(ws + WS_CSQ + (bid * 128 + nl) * 4, csum); }
        BAR_LGKM();
    }
    {
        bf16x4 xc[2], wc[NB];
#pragma unroll
        for (int tb = 0; tb < 2; ++tb) xc[tb] = lds64(tab + T_XC + (64 * wm + 32 * (FIRST ? tb ^ wn : tb) + r) * 16 + h * 8);
#pragma unroll
        for (int j = 0; j < NB; ++j) wc[j] = lds64(tab + T_WC + ((NB * wn + (FIRST ? j ^ wm : j)) * 32 + r) * 16 + h * 8);
#pragma unroll
        for (int j = 0; j < NB; ++j)
#pragma unroll
            for (int tb = 0; tb < 2; ++tb) acc[j][tb] = SWAP ? MFMA32K8(wc[j], xc[tb], acc[j][tb]) : MFMA32K8(xc[tb], wc[j], acc[j][tb]);
    }
#undef LN_STEP
#undef LN_READ
#undef LN_MMA
}

struct Wts { const char *wo, *wg, *wu, *wd; };
constexpr int QKV_BASE = 32768, RS_SLOT = 18432;
DI void ln_tile(int mode, int bid, int& tm, int& tn) {
    const int xcd = bid & 7, bi = bid >> 3, pr = xcd >> 1, e = xcd & 1;
    if (mode == 0) { const int lin = e * 30 + bi; tm = 4 * pr + (lin & 3); tn = lin >> 2; }
    else { tm = 4 * pr + (bi & 3); tn = 8 * e + (bi >> 2); }
}
DI unsigned wqkv_off(int piece, int tn, int lane) { const int k = piece * 4 + (lane >> 4), g = lane & 15; return (unsigned)(k * 3840 + tn * 256 + (((g >> 2) ^ (k & 3)) << 6) + (g & 3) * 16); }
template <int NW>
DI void prefetch_res(unsigned sm0, const char* wtile, int slot0, int j, unsigned l16) {
#pragma unroll
    for (int i = 0; i < (20 + NW - 1) / NW; ++i) { const int pc = j + NW * i, st = pc >= 10;
        if (pc < 20) glds16s<true>(wtile + pc * 1024, l16, sm0 + (slot0 + st) * RS_SLOT + 8192 + (pc - 10 * st) * 1024); }
}

template <int MODE, bool FIRST, bool MEGA>
DI void phase_ln(LAS char* smem, const char* X, const char* W0, const char* stats, char* out0, char* out1, char* ws, const Wts& wt,
                 int cin, int cout, unsigned& tag, int wave DBG_ARG) {
    using C = LnCfg<MODE>;
    PHASE_IDS();
    const unsigned sm0 = (unsigned)(size_t)smem;
    int tm, tn;
    ln_tile(MODE, bid, tm, tn);
    LAS char* tab = smem + C::SLOT;

    if (MODE == 0 && bi >= 30) {
        if (FIRST) {
            const int blk = (xcd * 2 + bi - 30) * 3 + (tid >> 8), m = blk / 24, c = tid & 7, rg = (tid & 255) >> 3;
            const char* p = (m ? wt.wu : wt.wg) + rg * 3072 + (blk - 24 * m) * 128 + c * 16;
            float s[8];
#pragma unroll
            for (int i = 0; i < 8; ++i) s[i] = 0.f;
            u32x4 v[20];
#pragma unroll
            for (int j = 0; j < 20; ++j) v[j] = *(const u32x4*)(p + j * 32 * 3072);
#pragma unroll
            for (int j = 0; j < 20; ++j)
#pragma unroll
                for (int i = 0; i < 4; ++i) { s[2 * i] += bflo(v[j][i]); s[2 * i + 1] += bfhi(v[j][i]); }
            LAS float* red = (LAS float*)smem;
#pragma unroll
            for (int i = 0; i < 8; ++i) red[rg * 192 + (tid >> 8) * 64 + c * 8 + i] = s[i];
            BAR_LGKM();
            if (tid < 192) {
                float t = 0.f;
#pragma unroll
                for (int i = 0; i < 32; ++i) t += red[i * 192 + tid];
                const int b2 = (xcd * 2 + bi - 30) * 3 + (tid >> 6), m2 = b2 / 24, f = (b2 - 24 * m2) * 64 + (tid & 63), tf = f / 96, fl = f - tf * 96;
                st32<MEGA>(ws + WS_CSGU + (tf * 192 + (fl >> 4) * 32 + m2 * 16 + (fl & 15)) * 4, t);
            }
            if (MEGA) { tag = get_tag(ws); WAITV(0); }
        }
        return;
    }

    if (w < 8) {
        const int j = w;
        unsigned off[C::PPW];
#pragma unroll
        for (int p = 0; p < 2; ++p) {
            if (FIRST) { const int row = (j + 8 * p) * 8 + (lane >> 3); off[p] = (unsigned)((tm * 128 + row) * 1280 + (((lane & 7) ^ ((row >> 1) & 7)) << 4)); }
            else off[p] = (unsigned)((2 * tm + p) * 81920 + j * 1024 + lane * 16);
        }
#pragma unroll
        for (int p = 2; p < C::PPW; ++p) {
            const int piece = j + 8 * (p - 2);
            if (MODE == 0) off[p] = wqkv_off(piece, tn, lane);
            else off[p] = (unsigned)(tn * 245760 + piece * 1024 + lane * 16);
        }
        constexpr int XSTAGE = FIRST ? 128 : 8192, WSTAGE = MODE == 0 ? 64 * 3840 : 24576;
        int kb = FIRST ? (3 * (tm & 3) + 7 * tn) % 10 : 0;
        constexpr bool WDEV = MEGA && MODE == 1;
        auto issue = [&](int s) {
            const unsigned sb = sm0 + ((s + 2) & 3) * C::SLOT + j * 1024;
#pragma unroll
            for (int p = 0; p < 2; ++p) glds16s<MEGA && !FIRST>(X + kb * XSTAGE, off[p], sb + p * 8192);
#pragma unroll
            for (int p = 2; p < C::PPW; ++p) glds16s<WDEV>(W0 + kb * WSTAGE, off[p], sb + p * 8192);
            kb = (kb == 9) ? 0 : kb + 1;
        };
        if (FIRST) {
            issue(0); issue(1);
            WAITV(C::PPW);
            BAR_RAW();
#pragma unroll 1
            for (int s = 0; s < 8; ++s) { issue(s + 2); WAITV(C::PPW); BAR_RAW(); }
            WAITV(0);
            BAR_RAW();
            if (MEGA) tag = get_tag(ws);
            BAR_RAW();
        } else {
            if (MEGA) {
#pragma unroll
                for (int p = 0; p < C::PPW; ++p) PIN(off[p]);
                BAR_RAW();
#pragma unroll
                for (int s = 0; s < 2; ++s)
#pragma unroll
                    for (int p = 0; p < 2; ++p) glds16s<true>(X + s * XSTAGE, off[p], sm0 + (s + 2) * C::SLOT + j * 1024 + p * 8192);
                kb = 2;
            } else { issue(0); issue(1); }
            asm volatile("" ::: "memory");
            const char* sp = stats + (2 * tm + (tid >> 8)) * 4096 + (tid & 3) * 1024 + ((tid >> 2) & 63) * 8;
            const f32x2 st0 = __builtin_bit_cast(f32x2, ld64<MEGA>(sp)), st1 = __builtin_bit_cast(f32x2, ld64<MEGA>(sp + 512));
            const float cs = ld32<MEGA>(ws + (MODE == 0 ? WS_CSQ + bid * 512 : WS_CSGU + tn * 768) + (tid < C::NCS ? tid : 0) * 4);
            WAITV((MEGA ? 2 : C::PPW) + 3);
            BAR_RAW();
#pragma unroll 1
            for (int s = 0; s < 8; ++s) { issue(s + 2); WAITV(C::PPW); BAR_RAW(); }
            if (MEGA && MODE == 1) {
                prefetch_res<8>(sm0, ws + WS_WD + (4 * e + (bi >> 3)) * 245760, 0, j, lane * 16);
                if (j < 4) WAITV(3); else WAITV(2);
            } else WAITV(0);
            BAR_RAW();
            const float mu = quad_sum(st0[0] + st1[0]) * 0.125f, d0 = st0[0] - mu, d1 = st1[0] - mu;
            const float m2 = quad_sum(st0[1] + st1[1] + 80.f * (d0 * d0 + d1 * d1));
            if ((tid & 3) == 0) { *(LAS u32x4*)(tab + T_XC + (tid >> 2) * 16) = split3(-mu, true);
                                  *(LAS float*)(tab + T_RS + (tid >> 2) * 4) = __builtin_amdgcn_rsqf(m2 * (1.f / DM) + LN_EPS); }
            if (tid < C::NCS) *(LAS u32x4*)(tab + T_WC + tid * 16) = split3(cs, false);
            BAR_LGKM();
        }
        return;
    }

    const int wm = w & 1, wn = (w >> 1) & 1, r = lane & 31, h = lane >> 5;
    LAS const float* trs = (LAS const float*)(tab + T_RS);
    if (MEGA && FIRST) tag = get_tag(ws);
    unsigned long long tb0 = 0;
    f32x16 acc[C::NB][2];
#pragma unroll
    for (int j = 0; j < C::NB; ++j)
#pragma unroll
        for (int tb = 0; tb < 2; ++tb)
#pragma unroll
            for (int i = 0; i < 16; ++i) acc[j][tb][i] = 0.f;

    if (MODE == 1) {
        STAMPW(1, 8); ln_mainloop<MODE, FIRST, true, MEGA>(smem, acc, wm, wn, lane, ws, tm, bid, cin, tag, tb0 DBG_PASS(kid)); STAMPW(2, 8); STAMPV(11, 8, tb0);
        char* hp = out0 + (unsigned)(((2 * tm + wm) * 192 + tn * 12 + 6 * wn + h) * 1024 + r * 16);
#pragma unroll
        for (int tb = 0; tb < 2; ++tb) {
            const float rs = trs[64 * wm + 32 * tb + r], rs2 = rs * rs;
#pragma unroll
            for (int j = 0; j < 3; ++j) {
                unsigned pk[2][2];
#pragma unroll
                for (int qd = 0; qd < 2; ++qd) {
                    float hv[4];
#pragma unroll
                    for (int i = 0; i < 4; ++i) hv[i] = fmaxf(acc[j][tb][4 * qd + i] * rs2, 0.f) * acc[j][tb][8 + 4 * qd + i];
                    pk[qd][0] = pack2(hv[0], hv[1]); pk[qd][1] = pack2(hv[2], hv[3]);
                }
                auto s0 = __builtin_amdgcn_permlane32_swap(pk[0][0], pk[1][0], false, false);
                auto s1 = __builtin_amdgcn_permlane32_swap(pk[0][1], pk[1][1], false, false);
                st128<MEGA>(hp + j * 2048 + tb * 512, (u32x4){s0[0], s1[0], s0[1], s1[1]});
                SBAR();
            }
        }
    } else if (tn < 10) {
        STAMPW(1, 8); ln_mainloop<MODE, FIRST, true, MEGA>(smem, acc, wm, wn, lane, ws, tm, bid, cin, tag, tb0 DBG_PASS(kid)); STAMPW(2, 8); STAMPV(11, 8, tb0);
        const float sc = tn < 5 ? QSCALE : 1.f;
        char* qp = out0 + (unsigned)(((2 * tm + wm) * 160 + tn * 16 + 8 * wn + h) * 1024 + r * 16);
#pragma unroll
        for (int tb = 0; tb < 2; ++tb) {
            const int tba = FIRST ? tb ^ wn : tb;
            const float rs = trs[64 * wm + 32 * tba + r] * sc;
#pragma unroll
            for (int cb = 0; cb < 2; ++cb)
#pragma unroll
                for (int qp2 = 0; qp2 < 2; ++qp2) {
                    const f32x16& a = acc[cb][tb];
                    auto s0 = __builtin_amdgcn_permlane32_swap(pack2(a[8 * qp2] * rs, a[8 * qp2 + 1] * rs), pack2(a[8 * qp2 + 4] * rs, a[8 * qp2 + 5] * rs), false, false);
                    auto s1 = __builtin_amdgcn_permlane32_swap(pack2(a[8 * qp2 + 2] * rs, a[8 * qp2 + 3] * rs), pack2(a[8 * qp2 + 6] * rs, a[8 * qp2 + 7] * rs), false, false);
                    st128<MEGA>(qp + (4 * (FIRST ? cb ^ wm : cb) + 2 * qp2) * 1024 + tba * 512, (u32x4){s0[0], s1[0], s0[1], s1[1]});
                    SBAR();
                }
        }
    } else {
        STAMPW(1, 8); ln_mainloop<MODE, FIRST, false, MEGA>(smem, acc, wm, wn, lane, ws, tm, bid, cin, tag, tb0 DBG_PASS(kid)); STAMPW(2, 8); STAMPV(11, 8, tb0);
        char* vp = out1 + (unsigned)((((tm * 8 + 4 * wm) * 2 + h) * 640 + (tn - 10) * 128 + 64 * wn + r) * 16);
#pragma unroll
        for (int tb = 0; tb < 2; ++tb)
#pragma unroll
            for (int s = 0; s < 2; ++s) {
                const int tba = FIRST ? tb ^ wn : tb, tl = 64 * wm + 32 * tba + 16 * s + 4 * h;
                const f32x4 r0 = *(LAS const f32x4*)(trs + tl), r1 = *(LAS const f32x4*)(trs + tl + 8);
#pragma unroll
                for (int cb = 0; cb < 2; ++cb) {
                    const f32x16& a = acc[cb][tb];
                    st128<MEGA>(vp + (2 * tba + s) * 20480 + (FIRST ? cb ^ wm : cb) * 512,
                          (u32x4){pack2(a[8 * s] * r0[0], a[8 * s + 1] * r0[1]), pack2(a[8 * s + 2] * r0[2], a[8 * s + 3] * r0[3]),
                                  pack2(a[8 * s + 4] * r1[0], a[8 * s + 5] * r1[1]), pack2(a[8 * s + 6] * r1[2], a[8 * s + 7] * r1[3])});
                    SBAR();
                }
            }
    }
    RT(ts);
    if (MEGA) signal(ws, tag, cout + (MODE == 1 ? tm * 16 + tn : tn * 16 + tm), 2 * wm + wn, lane);
    STAMPV(10, 8, ts);
}

template <int KT, bool MEGA>
DI void phase_res(LAS char* smem, const char* A, const char* W, const char* res, char* out, char* stats, char* ws, bool resrow, bool outrow,
                  u32x2 (&rv)[5], int cin, int cout, bool wready, const char* wnext, unsigned tag, int wave DBG_ARG) {
    constexpr int NS = KT / 64, NSLOT = 5, SLOT = RS_SLOT, DEP = 3, SL0 = KT == DM ? 3 : 0;
    constexpr bool TAILS = KT == DM;
    PHASE_IDS();
    const int tm = 8 * pr + (bi & 7), tn = 4 * e + (bi >> 3);

    if (w < 8) {
        const int j = w;
        const unsigned off = (unsigned)(j * 1024 + lane * 16);
        const char* ab = A + tm * (KT * 128);
        const char* wb = W + tn * (NS * 10240);
        const unsigned sm0 = (unsigned)(size_t)smem;
        unsigned sb = sm0 + SL0 * SLOT + j * 1024;
        int sl = SL0, ntm, ntn;
        ln_tile(TAILS ? 1 : 0, bid, ntm, ntn);
        const unsigned qo0 = wqkv_off(j, ntn, lane), qo1 = wqkv_off(j + 8, ntn, lane);
        auto issue = [&]() {
            glds16s<MEGA>(ab, off, sb);
            glds16s<MEGA>(wb, off, sb + 8192);
            if (j < 2) glds16s<MEGA>(wb + 8192, off, sb + 16384);
            ab += 8192; wb += 10240;
            if (sl == NSLOT - 1) { sl = 0; sb -= (NSLOT - 1) * SLOT; } else { ++sl; sb += SLOT; }
        };
#define RS_WAIT(n, x) do { if (j < 2) WAITV(3 * (n) + (x)); else WAITV(2 * (n) + (x)); } while (0)
        if (MEGA) BAR_RAW();
        if (MEGA && wready) {
            glds16s<true>(ab, off, sb); glds16s<true>(ab + 8192, off, sb + SLOT);
            ab += 2 * 8192; wb += 2 * 10240; sl = (SL0 + 2) % NSLOT; sb = sm0 + sl * SLOT + j * 1024;
            issue();
            RS_WAIT(1, 1);
        } else {
            issue(); issue(); issue();
            RS_WAIT(2, 0);
        }
        BAR_RAW();
#pragma unroll 1
        for (int s = 0; s < NS - DEP; ++s) { issue(); RS_WAIT(2, 0); BAR_RAW(); }
        if (MEGA && TAILS) {
#pragma unroll
            for (int s = 0; s < 2; ++s)
#pragma unroll
                for (int p = 0; p < 3; ++p) glds16s<true>(wnext + ntn * 245760 + s * 24576 + p * 8192, off, sm0 + (s + 2) * 40960 + 16384 + p * 8192 + j * 1024);
            RS_WAIT(1, 6); BAR_RAW(); WAITV(6); BAR_RAW();
        } else if (MEGA && wnext && bi < 30) {
#pragma unroll
            for (int s = 0; s < 2; ++s) {
                glds16s<false>(wnext + s * (64 * 3840), qo0, sm0 + QKV_BASE + (s + 2) * 32768 + 16384 + j * 1024);
                glds16s<false>(wnext + s * (64 * 3840), qo1, sm0 + QKV_BASE + (s + 2) * 32768 + 24576 + j * 1024);
            }
            RS_WAIT(1, 4); BAR_RAW(); WAITV(4); BAR_RAW();
        } else { RS_WAIT(1, 0); BAR_RAW(); WAITV(0); BAR_RAW(); }
#undef RS_WAIT
        return;
    }

    const int cw = w - 8, r = lane & 15, g = lane >> 4, row = 16 * cw + r;
    if (resrow) {
#pragma unroll
        for (int j = 0; j < 5; ++j) rv[j] = *(const u32x2*)(res + (unsigned)((tm * 64 + row) * 1280 + tn * 160 + j * 32 + g * 8));
    } else if (!MEGA) {
#pragma unroll
        for (int j = 0; j < 5; ++j) rv[j] = *(const u32x2*)(res + (unsigned)((tm * 80 + tn * 10 + 2 * j + (g >> 1)) * 1024 + row * 16 + (g & 1) * 8));
    }
    const unsigned xa = g * 1024 + row * 16, xt = g * 1024 + row * 8, wa = 8192 + lane * 16;
    f32x4 acc[5];
#pragma unroll
    for (int j = 0; j < 5; ++j) acc[j] = (f32x4){0.f, 0.f, 0.f, 0.f};
    bf16x8 xf[2], wf[2][5];
    if (MEGA) {
        if (w == 8) { if (TAILS) await<2>(ws, tag, cin + (tm >> 2) * 32 + (lane & 31), 2 * (tm & 3)); else await<2>(ws, tag, cin + (tm >> 1) * 16 + (lane & 15), 2 * (tm & 1)); }
        if (!wready) await<4>(ws, tag, CN_R + cw * 64 + lane, 0);
        RT(tp);
        BAR_RAW();
        RT(tg); STAMPV(8, 8, tp); STAMPV(9, 8, tg);
    }
    BAR_RAW();
    STAMPW(1, 8);
    { RT(tb0); STAMPV(11, 8, tb0); }
#pragma unroll
    for (int ks = 0; ks < 2; ++ks) {
        xf[ks] = lds128(smem + SL0 * SLOT + xa + ks * 4096);
#pragma unroll
        for (int j = 0; j < 5; ++j) wf[ks][j] = lds128(smem + SL0 * SLOT + wa + ks * 5120 + j * 1024);
    }
    int slot = SL0;
#define RS_STAGE(TAIL) do { \
        slot = (slot + 1 == NSLOT) ? 0 : slot + 1; \
        LAS char* nb = smem + slot * SLOT; \
        BAR_RAW();                                   \
        SBAR(); \
        _Pragma("unroll") for (int ks = 0; ks < 2; ++ks) { \
            _Pragma("unroll") for (int j = 0; j < 5; ++j) { \
                acc[j] = MFMA16(wf[ks][j], xf[ks], acc[j]); \
                wf[ks][j] = lds128(nb + wa + ks * 5120 + j * 1024); \
                if (j == 4) xf[ks] = (TAIL) ? cat(lds64(nb + xt + ks * 4096), lds64(nb + xt + ks * 4096 + 512)) : lds128(nb + xa + ks * 4096); \
                SBAR(); \
            } } } while (0)
#pragma unroll 1
    for (int s = 0; s < NS - 1 - (TAILS ? 2 : 0); ++s) RS_STAGE(false);
    if (TAILS) {
#pragma unroll 1
        for (int s = 0; s < 2; ++s) RS_STAGE(true);
    }
#undef RS_STAGE
#pragma unroll
    for (int ks = 0; ks < 2; ++ks)
#pragma unroll
        for (int j = 0; j < 5; ++j) acc[j] = MFMA16(wf[ks][j], xf[ks], acc[j]);
    STAMPW(2, 8);
#pragma unroll
    for (int j = 0; j < 5; ++j)
        rv[j] = (u32x2){pack2(acc[j][0] + bflo(rv[j][0]), acc[j][1] + bfhi(rv[j][0])), pack2(acc[j][2] + bflo(rv[j][1]), acc[j][3] + bfhi(rv[j][1]))};
    if (outrow) {
#pragma unroll
        for (int j = 0; j < 5; ++j) st64<false>(out + (unsigned)((tm * 64 + row) * 1280 + tn * 160 + j * 32 + g * 8), rv[j]);
    } else {
        char* op = out + (unsigned)((tm * 80 + tn * 10) * 1024 + row * 16);
#pragma unroll
        for (int jp = 0; jp < 2; ++jp) {
            auto s0 = __builtin_amdgcn_permlane16_swap(rv[2 * jp][0], rv[2 * jp + 1][0], false, false);
            auto s1 = __builtin_amdgcn_permlane16_swap(rv[2 * jp][1], rv[2 * jp + 1][1], false, false);
            st128<MEGA>(op + (4 * jp + 2 * (g & 1) + (g >> 1)) * 1024, (u32x4){s0[0], s1[0], s0[1], s1[1]});
        }
        st64<MEGA>(op + (8 + (g >> 1)) * 1024 + (g & 1) * 8, rv[4]);
        float v[20]; float sum = 0.f;
#pragma unroll
        for (int j = 0; j < 5; ++j) {
            v[4 * j] = bflo(rv[j][0]); v[4 * j + 1] = bfhi(rv[j][0]); v[4 * j + 2] = bflo(rv[j][1]); v[4 * j + 3] = bfhi(rv[j][1]);
            sum += (v[4 * j] + v[4 * j + 1]) + (v[4 * j + 2] + v[4 * j + 3]);
        }
        const float mean = xsum16_32(sum) * (1.f / 80.f);
        float m2 = 0.f;
#pragma unroll
        for (int i = 0; i < 20; ++i) { const float d = v[i] - mean; m2 += d * d; }
        m2 = xsum16_32(m2);
        if (g == 0) st64<MEGA>(stats + (unsigned)((tm * 8 + tn) * 512 + row * 8), (u32x2){__float_as_uint(mean), __float_as_uint(m2)});
        RT(ts);
        if (MEGA) signal(ws, tag, cout + tm * 8 + tn, cw, lane);
        STAMPV(10, 8, ts);
    }
}

constexpr int ACH = 23552, A_V = 12288, VSTEP = 704, VHALF = 352, ANSL = 6, A_Q = ANSL * ACH;
DI float swap_max(float m) { auto rr = __builtin_amdgcn_permlane32_swap(__float_as_uint(m), __float_as_uint(m), false, false); return fmaxf(__uint_as_float(rr[0]), __uint_as_float(rr[1])); }
DI float max16(const f32x16& s) {
    float a = fmaxf(fmaxf(s[0], s[1]), s[2]), b = fmaxf(fmaxf(s[3], s[4]), s[5]);
    a = fmaxf(fmaxf(a, s[6]), s[7]); b = fmaxf(fmaxf(b, s[8]), s[9]);
    a = fmaxf(fmaxf(a, s[10]), s[11]); b = fmaxf(fmaxf(b, s[12]), s[13]);
    a = fmaxf(fmaxf(a, s[14]), s[15]);
    return swap_max(fmaxf(a, b));
}
DI bf16x8 packp(const f32x16& p, int s) {
    u32x4 u = {pack2(p[8 * s], p[8 * s + 1]), pack2(p[8 * s + 2], p[8 * s + 3]), pack2(p[8 * s + 4], p[8 * s + 5]), pack2(p[8 * s + 6], p[8 * s + 7])};
    return __builtin_bit_cast(bf16x8, u);
}

DI void retile_slab(int task, LAS char* scr, const Wts& wt, char* ws, int lane) {
    const unsigned sl = (unsigned)(size_t)scr, l16 = lane * 16;
    const int q = (lane & 15) >> 2, pp = lane & 3;
    if (task < 544) {
        const bool iso = task < 160;
        const int t2 = iso ? task : task - 160, nk = iso ? 20 : 48, ct = t2 / nk, ks = t2 - ct * nk;
#pragma unroll
        for (int p = 0; p < 5; ++p) { const int G = p * 64 + lane, kr = G / 10, c = G - kr * 10, k = ks * 32 + kr;
            const int row = !iso ? k : (k < 512 ? 20 * (k >> 4) + (k & 15) : 20 * ((k - 512) >> 2) + 16 + (k & 3));
            glds16s<false>(iso ? wt.wo : wt.wd, (unsigned)(row * 1280 + ct * 160 + c * 16), sl + p * 1024); }
        WAITV(0);
        LAS const char* rp = scr + (8 * (lane >> 4) + q) * 160 + pp * 8;
#pragma unroll
        for (int i = 0; i < 5; ++i) st128s(ws + (iso ? WS_WO : WS_WD) + t2 * 5120 + i * 1024, l16, __builtin_bit_cast(u32x4, cat(ldstr(rp + i * 32), ldstr(rp + 640 + i * 32))));
    } else if (task < 1184) {
        const int t2 = task - 544, tf = t2 / 40, ks = t2 - tf * 40;
#pragma unroll
        for (int p = 0; p < 3; ++p) { const int G = p * 64 + lane, k = G / 12, c = G - k * 12; const unsigned so = (unsigned)((ks * 16 + k) * 3072 + tf * 192 + c * 16);
            glds16s<false>(wt.wg, so, sl + p * 1024); glds16s<false>(wt.wu, so, sl + 3072 + p * 1024); }
        WAITV(0);
        LAS const char* rp = scr + ((lane >> 4) & 1) * 3072 + (8 * (lane >> 5) + q) * 192 + pp * 8;
#pragma unroll
        for (int i = 0; i < 6; ++i) st128s(ws + WS_WGU + t2 * 6144 + i * 1024, l16, __builtin_bit_cast(u32x4, cat(ldstr(rp + i * 32), ldstr(rp + 768 + i * 32))));
    }
}

template <bool MEGA>
DI void phase_attn(LAS char* smem, const char* QK, const char* V, char* O, char* ws, const Wts& wt, bool first, int cin, int cout, unsigned tag, int wave DBG_ARG) {
    PHASE_IDS();
    const int head = 16 * e + (bi >> 1), qt = 2 * pr + (bi & 1), par = head & 1;
    const unsigned sm0 = (unsigned)(size_t)smem, l16 = lane * 16;
    const int g0 = 5 * (head >> 1) + 3 * par, gt = 5 * (head >> 1) + 2;
    auto kq_src = [&](int sec, int blk256, int i) { const int grp = i / 3, part = i - 3 * grp; return QK + ((blk256 * 4 + grp) * 160 + sec * 80 + (part == 2 ? gt : g0 + part)) * 1024; };
    auto v_off = [&](int i, bool& ok) { const int G = i * 64 + lane, sh = G / 22, row = G - sh * 22; ok = row < 20; return (unsigned)((sh * 640 + head * 20 + row) * 16); };
    const int c0 = 2 * pr;
    if (MEGA) {
        if (w == 8) { const int sec = min(lane >> 3, 2), ct = (head * 20 + ((lane >> 2) & 1) * 19) >> 7;
                      await<4>(ws, tag, cin + (5 * sec + ct) * 16 + (sec ? 2 * c0 + (lane & 3) : 2 * qt + (lane & 1)), 0); }
        RT(tp);
        BAR_RAW();
        RT(tg); STAMPV(8, 8, tp); STAMPV(9, 8, tg);
    }
    {
        bool ok; const unsigned vo = v_off(w, ok);
        glds16s<MEGA>(kq_src(1, c0, w), l16, sm0 + w * 1024);
        glds16s<MEGA>(kq_src(0, qt, w), l16, sm0 + A_Q + w * 1024);
        if (w < 11 && ok) glds16s<MEGA>(V + c0 * 327680, vo, sm0 + A_V + w * 1024);
        if (tid < ANSL * 64) {
            const unsigned cv = (tid & 1) ? 0u : 0x3F803F80u;
            *(LAS u32x4*)(smem + (tid >> 6) * ACH + A_V + ((tid & 63) >> 1) * VHALF + 320 + (tid & 1) * 16) = (u32x4){cv, cv, cv, cv};
        }
    }
    if (w >= 8) {
        const int lw = w - 8;
        __builtin_amdgcn_s_setprio(3);
        unsigned vo[3]; bool ok[3];
#pragma unroll
        for (int i = 0; i < 3; ++i) vo[i] = v_off(lw + 4 * i, ok[i]);
        auto chunk = [&](int c) {
            const int cc = (c + c0) & 7; const unsigned sb = sm0 + (c >= ANSL ? c - ANSL : c) * ACH;
#pragma unroll
            for (int i = 0; i < 3; ++i) glds16s<MEGA>(kq_src(1, cc, lw + 4 * i), l16, sb + (lw + 4 * i) * 1024);
#pragma unroll
            for (int i = 0; i < 3; ++i) if ((i < 2 || lw < 3) && ok[i]) glds16s<MEGA>(V + cc * 327680, vo[i], sb + A_V + (lw + 4 * i) * 1024);
        };
        asm volatile("s_waitcnt vmcnt(0) lgkmcnt(0)\n\ts_barrier" ::: "memory");
        chunk(1);
        if (MEGA) await<4>(ws, tag, cin + (5 + (lane >> 5) * 5 + ((head * 20 + ((lane >> 4) & 1) * 19) >> 7)) * 16 + (lane & 15), 0);
        chunk(2);
        WAITV(0); BAR_RAW();
        chunk(3); chunk(4); chunk(5);
        WAITV(0); BAR_RAW();
        chunk(6); chunk(7);
        if (first) {
            if (lw < 3) retile_slab(bid + 256 * lw, smem + A_Q + lw * 6144, wt, ws, lane);
            WAITV(0); BAR_RAW();
            if (lw >= 2) retile_slab(bid + 256 * (lw + 1), smem + 2 * ACH + lw * 6144, wt, ws, lane);
            if (MEGA) signal(ws, tag, CN_R + bid, lw, lane);
        } else {
            WAITV(0);
            const int t = tid - 512;
            unsigned tv = *(const unsigned*)(ws + WS_WO + e * 409600 + bi * 12800 + min(t, 99) * 128);
            tv ^= *(const unsigned*)(ws + WS_WGU + e * 1966080 + bi * 61440 + t * 128);
            tv ^= *(const unsigned*)(ws + WS_WGU + e * 1966080 + bi * 61440 + min(256 + t, 479) * 128);
            BAR_RAW();
            asm volatile("" ::"v"(tv));
            if (MEGA) { prefetch_res<4>(sm0, ws + WS_WO + (4 * e + (bi >> 3)) * 102400, 3, lw, l16); WAITV(0); }
        }
        __builtin_amdgcn_s_setprio(0);
        if (MEGA) BAR_RAW();
        return;
    }

    const int r = lane & 31, h = lane >> 5;
    const unsigned k0a = h * 1024 + r * 16, k1a = 2048 + r * 16 + par * 8, va = A_V + h * VHALF + min(r, 21) * 16;
    f32x16 zero, negm, o;
#pragma unroll
    for (int i = 0; i < 16; ++i) { zero[i] = 0.f; o[i] = 0.f; }
    bf16x8 v1 = {0, 0, 0, 0, 0, 0, 0, 0};
    u32x4 pw0 = {0, 0, 0, 0}, pw1 = {0, 0, 0, 0};
    float e0 = 0.f, e1 = 0.f, e2 = 0.f, e3 = 0.f;
    PIN(o); PIN(v1); PIN(pw0); PIN(pw1);
    asm volatile("s_waitcnt vmcnt(0) lgkmcnt(0)\n\ts_barrier" ::: "memory");
    STAMPW(1, 0);
    { RT(tb0); STAMPV(11, 0, tb0); }
    bf16x8 q0; bf16x4 q1;
    { LAS const char* qp = smem + A_Q + (w >> 1) * 3072 + (w & 1) * 512;
      q0 = lds128(qp + k0a); q1 = lds64(qp + k1a);
      if (h) q1 = (bf16x4){0, 0, 0, 0}; }
    LAS const char* pk0 = smem + k0a;
    LAS const char* pk1 = smem + k1a;
    LAS const char* pv = smem + va;
    f32x16 sA, sB;
    {
        const bf16x8 a = lds128(pk0); const bf16x4 b = lds64(pk1);
        f32x16 s = MFMA32(a, q0, zero);
        s = MFMA32K8(b, q1, s);
        const float m = max16(s);
#pragma unroll
        for (int i = 0; i < 16; ++i) negm[i] = -m;
        sA = MFMA32(a, q0, negm);
        sA = MFMA32K8(b, q1, sA);
    }
    bf16x8 ka = lds128(pk0 + 512), v0 = lds128(pv);
    bf16x4 kb = lds64(pk1 + 512);
#define EX(x) __builtin_amdgcn_exp2f(x)
#define ATT_STEP(SN, SC, PK0, PK1, PV0, PV1) do { \
        float t0, t1, t2, t3; \
        SN = MFMA32(ka, q0, negm); PIN(SN); ka = lds128(PK0); \
        pw1[2] = pack2(e0, e1); pw1[3] = pack2(e2, e3); PIN(pw1); SBAR(); \
        t0 = EX(SC[0]); t1 = EX(SC[1]); t2 = EX(SC[2]); t3 = EX(SC[3]); PIN(t0); PIN(t1); PIN(t2); PIN(t3); SBAR(); \
        o = MFMA32(v1, __builtin_bit_cast(bf16x8, pw1), o); PIN(o); v1 = lds128(PV1); \
        pw0[0] = pack2(t0, t1); pw0[1] = pack2(t2, t3); PIN(pw0); SBAR(); \
        t0 = EX(SC[4]); t1 = EX(SC[5]); t2 = EX(SC[6]); t3 = EX(SC[7]); PIN(t0); PIN(t1); PIN(t2); PIN(t3); SBAR(); \
        SN = MFMA32K8(kb, q1, SN); PIN(SN); kb = lds64(PK1); \
        pw0[2] = pack2(t0, t1); pw0[3] = pack2(t2, t3); PIN(pw0); SBAR(); \
        t0 = EX(SC[8]); t1 = EX(SC[9]); t2 = EX(SC[10]); t3 = EX(SC[11]); PIN(t0); PIN(t1); PIN(t2); PIN(t3); SBAR(); \
        o = MFMA32(v0, __builtin_bit_cast(bf16x8, pw0), o); PIN(o); v0 = lds128(PV0); \
        pw1[0] = pack2(t0, t1); pw1[1] = pack2(t2, t3); PIN(pw1); SBAR(); \
        e0 = EX(SC[12]); e1 = EX(SC[13]); e2 = EX(SC[14]); e3 = EX(SC[15]); PIN(e0); PIN(e1); PIN(e2); PIN(e3); SBAR(); \
    } while (0)
#define KOF(u) (((u) >> 1) * 3072 + ((u) & 1) * 512)
#pragma unroll 1
    for (int c = 0; c < 8; ++c) {
        const int step = (c == ANSL - 1) ? -(ANSL - 1) * ACH : ACH;
        LAS const char* nk0 = pk0 + step; LAS const char* nk1 = pk1 + step; LAS const char* nv = pv + step;
        if (c == 4) __builtin_amdgcn_s_setprio(2); else if (c == 0 || c == 2 || c == 5 || c == 7) __builtin_amdgcn_s_setprio(1);
        ATT_STEP(sB, sA, pk0 + KOF(2), pk1 + KOF(2), pv + 2 * VSTEP, pv + 1 * VSTEP);
        ATT_STEP(sA, sB, pk0 + KOF(3), pk1 + KOF(3), pv + 4 * VSTEP, pv + 3 * VSTEP);
        ATT_STEP(sB, sA, pk0 + KOF(4), pk1 + KOF(4), pv + 6 * VSTEP, pv + 5 * VSTEP);
        ATT_STEP(sA, sB, pk0 + KOF(5), pk1 + KOF(5), pv + 8 * VSTEP, pv + 7 * VSTEP);
        if (c == 1 || c == 6) __builtin_amdgcn_s_setprio(2); else if (c != 3 && c != 4) __builtin_amdgcn_s_setprio(0);
        ATT_STEP(sB, sA, pk0 + KOF(6), pk1 + KOF(6), pv + 10 * VSTEP, pv + 9 * VSTEP);
        ATT_STEP(sA, sB, pk0 + KOF(7), pk1 + KOF(7), pv + 12 * VSTEP, pv + 11 * VSTEP);
        if (c == 0 || c == 2 || c == 5) { BAR_RAW(); __builtin_amdgcn_s_setprio(3); }
        ATT_STEP(sB, sA, nk0, nk1, pv + 14 * VSTEP, pv + 13 * VSTEP);
        ATT_STEP(sA, sB, nk0 + KOF(1), nk1 + KOF(1), nv, pv + 15 * VSTEP);
        pk0 = nk0; pk1 = nk1; pv = nv;
    }
#undef KOF
#undef ATT_STEP
    pw1[2] = pack2(e0, e1); pw1[3] = pack2(e2, e3);
    o = MFMA32(v1, __builtin_bit_cast(bf16x8, pw1), o);
    STAMPW(2, 0);
    float l;
    { auto rr = __builtin_amdgcn_permlane32_swap(__float_as_uint(o[8]), __float_as_uint(o[8]), false, false); l = __uint_as_float(rr[1]); }
    float chk = 0.f;
#pragma unroll
    for (int i = 0; i < 12; ++i) chk += o[i] * 0.f;
    const bool okr = (chk == 0.f) && (l <= 1.8446744e19f);
    if (__builtin_expect(!__all(okr), 0)) {
        float m = -INFINITY;
#pragma unroll
        for (int i = 0; i < 16; ++i) o[i] = 0.f;
        const unsigned cv = r == 20 ? 0x3F803F80u : 0u;
#pragma unroll 1
        for (int kbk = 0; kbk < 64; ++kbk) {
            const char* kp = QK + ((kbk >> 1) * 160 + 80) * 1024 + ((kbk & 1) * 32 + r) * 16;
            const u32x2 ka0 = ld64<MEGA>(kp + (g0 + h) * 1024), ka1 = ld64<MEGA>(kp + (g0 + h) * 1024 + 8);
            f32x16 s = MFMA32(__builtin_bit_cast(bf16x8, (u32x4){ka0[0], ka0[1], ka1[0], ka1[1]}), q0, zero);
            s = MFMA32K8(__builtin_bit_cast(bf16x4, ld64<MEGA>(kp + gt * 1024 + par * 8)), q1, s);
            const float mn = fmaxf(m, max16(s));
            const float al = __builtin_amdgcn_exp2f(m - mn);
            m = mn;
#pragma unroll
            for (int i = 0; i < 16; ++i) { o[i] *= al; s[i] = __builtin_amdgcn_exp2f(s[i] - mn); }
#pragma unroll
            for (int st = 0; st < 2; ++st) {
                const char* vq = V + (((2 * kbk + st) * 2 + h) * 640 + head * 20 + min(r, 19)) * 16;
                const u32x2 va0 = ld64<MEGA>(vq), va1 = ld64<MEGA>(vq + 8);
                u32x4 vv = {va0[0], va0[1], va1[0], va1[1]};
                if (r >= 20) vv = (u32x4){cv, cv, cv, cv};
                o = MFMA32(__builtin_bit_cast(bf16x8, vv), packp(s, st), o);
            }
        }
        auto rr = __builtin_amdgcn_permlane32_swap(__float_as_uint(o[8]), __float_as_uint(o[8]), false, false); l = __uint_as_float(rr[1]);
    }
    {
        const float inv = 1.f / l;
        char* ob = O + (unsigned)((qt * 4 + (w >> 1)) * 81920);
        const int row = (w & 1) * 32 + r;
        auto s0 = __builtin_amdgcn_permlane32_swap(pack2(o[0] * inv, o[1] * inv), pack2(o[4] * inv, o[5] * inv), false, false);
        auto s1 = __builtin_amdgcn_permlane32_swap(pack2(o[2] * inv, o[3] * inv), pack2(o[6] * inv, o[7] * inv), false, false);
        st128<MEGA>(ob + (unsigned)((2 * head + h) * 1024 + row * 16), (u32x4){s0[0], s1[0], s0[1], s1[1]});
        if (h == 0) st64<MEGA>(ob + (unsigned)(65536 + head * 512 + row * 8), (u32x2){pack2(o[8] * inv, o[9] * inv), pack2(o[10] * inv, o[11] * inv)});
    }
    RT(ts);
    if (MEGA) { signal(ws, tag, cout + qt * 32 + head, w, lane); RT(td); STAMPV(12, 7, td); BAR_RAW(); }
    STAMPV(10, 0, ts); STAMPV(13, 7, ts);
#undef EX
}

#define SMEM_DECL(n) __shared__ __attribute__((aligned(1024))) char smem_[n]; LAS char* smem = (LAS char*)smem_
#define PH(k, wv, call) call
template <bool MEGA>
DI void run_phase(int ph, int layer, LAS char* smem, const char* x0, const char* wqkv, const Wts& wt, char* ws, char* out, u32x2 (&rv)[5], unsigned& tag, int wave) {
    if (ph == 0) PH(1, 8, (phase_ln<0, true, MEGA>(smem, x0, wqkv, nullptr, ws + WS_QK, ws + WS_V, ws, wt, 0, CN_Q, tag, wave DBG_PASS(1))));
    if (ph == 1) PH(6, 8, (phase_ln<0, false, MEGA>(smem + QKV_BASE, ws + WS_XB, wqkv, ws + WS_ST + SZ_ST, ws + WS_QK + 2 * SZ_X, ws + WS_V + SZ_X, ws, wt, CN_X + 256, CN_Q + 256, tag, wave DBG_PASS(6))));
    if (ph == 2) PH(2 + 5 * layer, 0, (phase_attn<MEGA>(smem, ws + WS_QK + layer * 2 * SZ_X, ws + WS_V + layer * SZ_X, ws + WS_O + layer * SZ_X, ws, wt, layer == 0,
                                       CN_Q + 256 * layer, CN_O + 256 * layer, tag, wave DBG_PASS(2 + 5 * layer))));
    if (ph == 3) PH(3 + 5 * layer, 8, (phase_res<DM, MEGA>(smem, ws + WS_O + layer * SZ_X, ws + WS_WO, layer ? ws + WS_XB : x0, ws + WS_XA + layer * SZ_X,
                                       ws + WS_ST + 2 * layer * SZ_ST, ws, layer == 0, false, rv, CN_O + 256 * layer, CN_X + 512 * layer, layer != 0, ws + WS_WGU, tag, wave DBG_PASS(3 + 5 * layer))));
    if (ph == 4) PH(4 + 5 * layer, 8, (phase_ln<1, false, MEGA>(smem, ws + WS_XA + layer * SZ_X, ws + WS_WGU, ws + WS_ST + 2 * layer * SZ_ST,
                                       ws + WS_H + layer * (SEQ * FF * 2), nullptr, ws, wt, CN_X + 512 * layer, CN_H + 256 * layer, tag, wave DBG_PASS(4 + 5 * layer))));
    if (ph == 5) PH(5 + 5 * layer, 8, (phase_res<FF, MEGA>(smem, ws + WS_H + layer * (SEQ * FF * 2), ws + WS_WD, ws + WS_XA + layer * SZ_X, layer ? out : ws + WS_XB,
                                       ws + WS_ST + SZ_ST, ws, false, layer != 0, rv, CN_H + 256 * layer, CN_X + 256, true, layer ? nullptr : wqkv, tag, wave DBG_PASS(5 + 5 * layer))));
}
__global__ __launch_bounds__(768) void k_mega(const char* x0, const char* wqkv, const char* wo, const char* wg, const char* wu, const char* wd,
                                              char* ws, char* out) {
    SMEM_DECL(LDS_BYTES);
    const Wts wt = {wo, wg, wu, wd};
    u32x2 rv[5];
#pragma unroll
    for (int j = 0; j < 5; ++j) rv[j] = (u32x2){0u, 0u};
    unsigned tag = 0;
    const int wave = __builtin_amdgcn_readfirstlane(threadIdx.x >> 6);
    if (wave == 11) {
        unsigned long long pc;
        asm volatile("s_getpc_b64 %0" : "=s"(pc));
        const int lane = threadIdx.x & 63;
        const GAS char* p = lane < 13 ? (const GAS char*)(pc & ~127ull) + min((int)(blockIdx.x >> 3) * 13 + lane, CODE_LINES - 1) * 128
                                      : (lane < 40 ? (const GAS char*)ws + (size_t)min(lane - 13, (int)(WS_CNT >> 21)) * (2u << 20)
                                                   : (const GAS char*)(lane < 42 ? out + (lane - 40) * (2u << 20) : (lane == 42 ? wo : (lane == 43 ? wd : (lane == 44 ? wg : wu)))));
        unsigned tv = *(const GAS unsigned*)p;
        asm volatile("" ::"v"(tv));
    }
    run_phase<true>(0, 0, smem, x0, wqkv, wt, ws, out, rv, tag, wave);
#pragma unroll 1
    for (int layer = 0; layer < 2; ++layer) {
        if (layer) run_phase<true>(1, layer, smem, x0, wqkv, wt, ws, out, rv, tag, wave);
        run_phase<true>(2, layer, smem, x0, wqkv, wt, ws, out, rv, tag, wave);
        run_phase<true>(3, layer, smem, x0, wqkv, wt, ws, out, rv, tag, wave);
        run_phase<true>(4, layer, smem, x0, wqkv, wt, ws, out, rv, tag, wave);
        run_phase<true>(5, layer, smem, x0, wqkv, wt, ws, out, rv, tag, wave);
    }
    if (blockIdx.x == 0 && wave == 8) __hip_atomic_store((GAS unsigned*)(ws + WS_EPOCH), tag, __ATOMIC_RELAXED, __HIP_MEMORY_SCOPE_AGENT);
}

extern "C" void kernel_launch(void* const* d_in, const int* in_sizes, int n_in, void* d_out, int out_size, void* d_ws, size_t ws_size,
                              hipStream_t stream) {
    const char* x0 = (const char*)d_in[0];
    const char* wqkv = (const char*)d_in[1];
    const char* wo = (const char*)d_in[2];
    const char* wg = (const char*)d_in[4];
    const char* wu = (const char*)d_in[5];
    const char* wd = (const char*)d_in[6];
    char* ws = (char*)d_ws;
    char* out = (char*)d_out;
    k_mega<<<256, 768, 0, stream>>>(x0, wqkv, wo, wg, wu, wd, ws, out);
}
```

```cpp
#include <hip/hip_runtime.h>
#include <hip/hip_bf16.h>

typedef short bf16x8 __attribute__((ext_vector_type(8)));
typedef short bf16x4 __attribute__((ext_vector_type(4)));
typedef float f32x16 __attribute__((ext_vector_type(16)));
typedef float f32x4 __attribute__((ext_vector_type(4)));
typedef float f32x2 __attribute__((ext_vector_type(2)));
typedef unsigned u32x4 __attribute__((ext_vector_type(4)));
typedef unsigned u32x2 __attribute__((ext_vector_type(2)));
typedef __bf16 bf2_t __attribute__((ext_vector_type(2)));
#define LAS __attribute__((address_space(3)))
#define GAS __attribute__((address_space(1)))
#define DI __device__ __forceinline__
#define MFMA32(a, b, c) __builtin_amdgcn_mfma_f32_32x32x16_bf16((a), (b), (c), 0, 0, 0)
#define MFMA32K8(a, b, c) __builtin_amdgcn_mfma_f32_32x32x8bf16_1k((a), (b), (c), 0, 0, 0)
#define MFMA4(a, b, c) __builtin_amdgcn_mfma_f32_4x4x4bf16_1k((a), (b), (c), 0, 0, 0)
#define MFMA16(a, b, c) __builtin_amdgcn_mfma_f32_16x16x32_bf16((a), (b), (c), 0, 0, 0)
#define WAITV(n) asm volatile("s_waitcnt vmcnt(%0)" ::"i"(n) : "memory")
#define BAR_LGKM() asm volatile("s_waitcnt lgkmcnt(0)\n\ts_barrier" ::: "memory")
#define BAR_RAW() asm volatile("s_barrier" ::: "memory")
#define PIN(x) asm volatile("" : "+v"(x))
#define SBAR() __builtin_amdgcn_sched_barrier(0)

constexpr int SEQ = 2048, DM = 640, NH = 32, DH = 20, FF = 1536;
constexpr float QSCALE = 0.22360679774997896f * 1.4426950408889634f;
constexpr float LN_EPS = 1e-5f;
constexpr int LDS_BYTES = 163840;
constexpr int CODE_LINES = 392;

constexpr unsigned SZ_X = SEQ * DM * 2, SZ_ST = 32 * 8 * 512;
constexpr unsigned WS_XA = 0;
constexpr unsigned WS_XB = WS_XA + 2 * SZ_X;
constexpr unsigned WS_O = WS_XB + SZ_X;
constexpr unsigned WS_V = WS_O + 2 * SZ_X;
constexpr unsigned WS_QK = WS_V + 2 * SZ_X;
constexpr unsigned WS_H = WS_QK + 4 * SZ_X;
constexpr unsigned WS_ST = WS_H + 2 * SEQ * FF * 2;
constexpr unsigned WS_WO = WS_ST + 3 * SZ_ST;
constexpr unsigned WS_WD = WS_WO + DM * DM * 2;
constexpr unsigned WS_WGU = WS_WD + FF * DM * 2;
constexpr unsigned WS_CSQ = WS_WGU + 2 * DM * FF * 2;
constexpr unsigned WS_CSGU = WS_CSQ + 131072;
constexpr unsigned WS_EPOCH = WS_CSGU + 16384, WS_CNT = WS_EPOCH + 256;
constexpr int CN_O = 0, CN_X = CN_O + 2 * 2048, CN_H = CN_X + 3 * 1024, CN_Q = CN_H + 2 * 1024, CN_R = CN_Q + 2 * 1024;

#define STAMPV(i, wv, val)
#define RT(x)
#define DBG_ARG
#define DBG_PASS(k)
#define STAMPW(i, wv)

#define EXP_ROT
#define NOSTAT 0
#define NOSLAB 0
#define PHASE_IDS() \
    int lane, w = wave, bid = __builtin_amdgcn_readfirstlane(blockIdx.x); \
    asm volatile("v_mbcnt_lo_u32_b32 %0, -1, 0\n\tv_mbcnt_hi_u32_b32 %0, -1, %0" : "=v"(lane)); asm volatile("" : "+s"(bid), "+s"(w)); \
    const int tid = w * 64 + lane, xcd = bid & 7, bi = bid >> 3, pr = xcd >> 1, e = xcd & 1
DI unsigned pack2(float a, float b) { f32x2 v = {a, b}; return __builtin_bit_cast(unsigned, __builtin_convertvector(v, bf2_t)); }
DI float bflo(unsigned u) { return __uint_as_float(u << 16); }
DI float bfhi(unsigned u) { return __uint_as_float(u & 0xffff0000u); }
DI unsigned long long sgpr64(const void* p) {
    return ((unsigned long long)(unsigned)__builtin_amdgcn_readfirstlane((int)((unsigned long long)p >> 32)) << 32) |
           (unsigned)__builtin_amdgcn_readfirstlane((int)(unsigned long long)p);
}
template <bool DEV>
DI void glds16s(const char* sb_, unsigned voff, unsigned ldsaddr) {
    const unsigned long long sbase = sgpr64(sb_);
    ldsaddr = __builtin_amdgcn_readfirstlane(ldsaddr);
    if (DEV) asm volatile("s_mov_b32 m0, %2\n\ts_nop 3\n\tglobal_load_lds_dwordx4 %1, %0 sc1" ::"s"(sbase), "v"(voff), "s"(ldsaddr) : "memory");
    else asm volatile("s_mov_b32 m0, %2\n\ts_nop 3\n\tglobal_load_lds_dwordx4 %1, %0" ::"s"(sbase), "v"(voff), "s"(ldsaddr) : "memory");
}
template <bool DEV> DI void st128(char* p, u32x4 v) {
    if (DEV) asm volatile("global_store_dwordx4 %0, %1, off sc1\n\ts_nop 1" ::"v"(p), "v"(v) : "memory"); else *(u32x4*)p = v;
}
template <bool DEV> DI void st64(char* p, u32x2 v) {
    if (DEV) asm volatile("global_store_dwordx2 %0, %1, off sc1" ::"v"(p), "v"(v) : "memory"); else *(u32x2*)p = v;
}
template <bool DEV> DI u32x2 ld64(const char* p) {
    if (DEV) return __builtin_bit_cast(u32x2, __hip_atomic_load((const GAS unsigned long long*)p, __ATOMIC_RELAXED, __HIP_MEMORY_SCOPE_AGENT));
    return *(const u32x2*)p;
}
DI void st128s(char* sb_, unsigned voff, u32x4 v) {
    asm volatile("s_nop 4\n\tglobal_store_dwordx4 %1, %2, %0 sc1\n\ts_nop 1" ::"s"(sgpr64(sb_)), "v"(voff), "v"(v) : "memory");
}
DI unsigned get_tag(char* ws) {
    return (unsigned)__builtin_amdgcn_readfirstlane((int)__hip_atomic_load((const GAS unsigned*)(ws + WS_EPOCH), __ATOMIC_RELAXED, __HIP_MEMORY_SCOPE_AGENT)) + 1u;
}
template <bool DEV> DI void st32(char* p, float v) {
    if (DEV) __hip_atomic_store((GAS float*)p, v, __ATOMIC_RELAXED, __HIP_MEMORY_SCOPE_AGENT); else *(float*)p = v;
}
template <bool DEV> DI float ld32(const char* p) {
    if (DEV) return __hip_atomic_load((const GAS float*)p, __ATOMIC_RELAXED, __HIP_MEMORY_SCOPE_AGENT);
    return *(const float*)p;
}
DI void signal(char* ws, unsigned tag, int word, int lane) {
    WAITV(0);
    asm volatile("" : "+s"(tag));
    if (lane == 0) __hip_atomic_store((GAS unsigned*)(ws + WS_CNT + word * 4), tag, __ATOMIC_RELAXED, __HIP_MEMORY_SCOPE_AGENT);
}
template <int NWD>
DI void await(char* ws, unsigned tag, int word) {
    const char* p = ws + WS_CNT + word * 4;
    for (unsigned spins = 0; spins < (1u << 20); ++spins) {
        bool ok;
        if (NWD == 2) { u32x2 v; asm volatile("global_load_dwordx2 %0, %1, off sc1\n\ts_waitcnt vmcnt(0)" : "=&v"(v) : "v"(p) : "memory"); ok = v[0] == tag && v[1] == tag; }
        else { u32x4 v; asm volatile("global_load_dwordx4 %0, %1, off sc1\n\ts_waitcnt vmcnt(0)" : "=&v"(v) : "v"(p) : "memory"); ok = v[0] == tag && v[1] == tag && v[2] == tag && v[3] == tag; }
        if (__all(ok)) break;
        __builtin_amdgcn_s_sleep(8);
    }
}
#define POLL_DONE(v0, v1) asm volatile("" ::"v"(v0), "v"(v1))
DI void await_bar(bool poller, char* ws, unsigned tag, int word, unsigned& v0, unsigned& v1) {
    v0 = 0u; v1 = 0u;
    if (poller) {
        const unsigned off = WS_CNT + word * 4;
        unsigned cnt = 1u << 19;
        asm volatile("s_nop 4\n\ts_waitcnt vmcnt(0)\n\t"
                     "global_load_dword %0, %3, %4 sc1\n\t"
                     "s_sleep 6\n"
                     "1:\n\t"
                     "global_load_dword %1, %3, %4 sc1\n\t"
                     "s_waitcnt vmcnt(1)\n\t"
                     "v_cmp_ne_u32 vcc, %5, %0\n\t"
                     "s_cbranch_vccz 2f\n\t"
                     "global_load_dword %0, %3, %4 sc1\n\t"
                     "s_waitcnt vmcnt(1)\n\t"
                     "v_cmp_ne_u32 vcc, %5, %1\n\t"
                     "s_cbranch_vccz 2f\n\t"
                     "s_sub_u32 %2, %2, 1\n\t"
                     "s_cbranch_scc0 1b\n"
                     "2:\n\t"
                     "s_barrier"
                     : "+v"(v0), "+v"(v1), "+s"(cnt) : "v"(off), "s"(sgpr64(ws)), "s"(tag) : "vcc", "scc", "memory");
    } else BAR_RAW();
}
DI bf16x8 lds128(LAS const char* p) { return *(LAS const bf16x8*)p; }
DI bf16x4 lds64(LAS const char* p) { return *(LAS const bf16x4*)p; }
DI bf16x4 ldstr(LAS const char* p) { return __builtin_bit_cast(bf16x4, __builtin_amdgcn_ds_read_tr16_b64_v4i16((LAS bf16x4*)p)); }
DI bf16x8 cat(bf16x4 a, bf16x4 b) { return __builtin_shufflevector(a, b, 0, 1, 2, 3, 4, 5, 6, 7); }
DI float quad_sum(float v) {
    v += __int_as_float(__builtin_amdgcn_mov_dpp(__float_as_int(v), 0xB1, 0xf, 0xf, true));
    return v + __int_as_float(__builtin_amdgcn_mov_dpp(__float_as_int(v), 0x4E, 0xf, 0xf, true));
}
DI float half_sum(float v) { auto a = __builtin_amdgcn_permlane32_swap(__float_as_uint(v), __float_as_uint(v), false, false); return __uint_as_float(a[0]) + __uint_as_float(a[1]); }
DI float xsum16_32(float v) {
    auto a = __builtin_amdgcn_permlane16_swap(__float_as_uint(v), __float_as_uint(v), false, false);
    return half_sum(__uint_as_float(a[0]) + __uint_as_float(a[1]));
}
DI u32x4 split3(float v, bool rowside) {
    const unsigned p1 = pack2(v, v); const float r1 = v - bflo(p1);
    const unsigned p2 = pack2(r1, r1); const float r2 = r1 - bflo(p2);
    const unsigned p3 = pack2(r2, r2);
    const unsigned l1 = p1 & 0xffffu, l2 = p2 & 0xffffu, h1 = p1 & 0xffff0000u, h2 = p2 & 0xffff0000u, h3 = p3 & 0xffff0000u;
    return rowside ? (u32x4){p1, l2 | h1, l2 | h3, 0u} : (u32x4){l1 | h2, l1 | h3, l2 | h1, 0u};
}
DI bf16x4 lo4(bf16x8 v) { return __builtin_shufflevector(v, v, 0, 1, 2, 3); }
DI bf16x4 hi4(bf16x8 v) { return __builtin_shufflevector(v, v, 4, 5, 6, 7); }

template <int MODE> struct LnCfg;
template <> struct LnCfg<0> { static constexpr int SLOT = 32768, NB = 2, PPW = 4, NCS = 128; };
template <> struct LnCfg<1> { static constexpr int SLOT = 40960, NB = 3, PPW = 5, NCS = 192; };
constexpr int T_XC = 0, T_WC = 2048, T_RS = 5120, T_RAW = 5632;

template <int MODE, bool FIRST, bool SWAP, bool MEGA>
DI void ln_mainloop(LAS char* smem, f32x16 (&acc)[LnCfg<MODE>::NB][2], int wm, int wn, int lane, char* ws, int tm, int bid, int cin, unsigned tag, unsigned long long& tb0 DBG_ARG) {
    using C = LnCfg<MODE>;
    constexpr int NB = C::NB;
    const int r = lane & 31, h = lane >> 5, q = (lane & 15) >> 2, pp = lane & 3, blk = (lane >> 4) & 1;
    unsigned xa[4], wa[2];
    if (FIRST) {
        const int row = 64 * wm + 32 * wn + r, sw = (row >> 1) & 7;
#pragma unroll
        for (int ks = 0; ks < 4; ++ks) xa[ks] = row * 128 + (((2 * ks + h) ^ sw) << 4);
    } else xa[0] = wm * 8192 + h * 1024 + r * 16;
    if (MODE == 0) {
#pragma unroll
        for (int cb = 0; cb < 2; ++cb) wa[cb] = 16384 + (8 * h + q) * 256 + (((2 * wn + (FIRST ? cb ^ wm : cb)) ^ q) * 64) + blk * 32 + pp * 8;
    } else { wa[0] = 16384 + wn * 3072 + lane * 16; wa[1] = 0; }
    bf16x8 xf[2][2], wf[2][NB];
    f32x4 sx = {0.f, 0.f, 0.f, 0.f}, sw_ = sx; f32x16 sxx;
#pragma unroll
    for (int i = 0; i < 16; ++i) sxx[i] = 0.f;
    const short sv = ((lane & 15) == ((lane >> 4) & 1)) ? (short)0x3F80 : (short)0;
    const bf16x8 sel = {sv, sv, sv, sv, sv, sv, sv, sv};
#define LN_READ(BUF, SB, KS) do { \
        _Pragma("unroll") for (int tb = 0; tb < 2; ++tb) xf[BUF][tb] = FIRST ? lds128((SB) + (xa[KS] ^ (tb * 4096))) : lds128((SB) + xa[0] + (KS) * 2048 + tb * 512); \
        _Pragma("unroll") for (int j = 0; j < NB; ++j) { \
            if (MODE == 0) wf[BUF][j] = cat(ldstr((SB) + wa[j] + (KS) * 4096), ldstr((SB) + wa[j] + (KS) * 4096 + 1024)); \
            else wf[BUF][j] = lds128((SB) + wa[0] + (KS) * 6144 + j * 1024); } \
    } while (0)
#define LN_MMA(BUF) do { \
        _Pragma("unroll") for (int j = 0; j < NB; ++j) _Pragma("unroll") for (int tb = 0; tb < 2; ++tb) \
            acc[j][tb] = SWAP ? MFMA32(wf[BUF][j], xf[BUF][tb], acc[j][tb]) : MFMA32(xf[BUF][tb], wf[BUF][j], acc[j][tb]); \
        if (FIRST && !NOSTAT) { sx = MFMA16(sel, xf[BUF][0], sx); sw_ = MFMA16(sel, wf[BUF][0], sw_); sxx = MFMA32(xf[BUF][0], xf[BUF][0], sxx); } \
    } while (0)
#define LN_STEP(RB, SB, KS, MB) do { LN_READ(RB, SB, KS); LN_MMA(MB); \
        _Pragma("unroll") for (int i_ = 0; i_ < 3; ++i_) { __builtin_amdgcn_sched_group_barrier(0x8, 1, 0); __builtin_amdgcn_sched_group_barrier(0x100, 2, 0); } \
        __builtin_amdgcn_sched_group_barrier(0x8, 2 * NB - 3 + (FIRST ? 3 : 0), 0); \
        SBAR(); } while (0)
    if (MEGA && !FIRST) {
#pragma unroll
        for (int j = 0; j < NB; ++j) { PIN(acc[j][0]); PIN(acc[j][1]); }
        PIN(xa[0]); PIN(wa[0]); if (MODE == 0) PIN(wa[1]);
        unsigned pv0, pv1;
        await_bar(wm + wn == 0, ws, tag, cin + 64 * tm + lane, pv0, pv1);
        WAITV(0); POLL_DONE(pv0, pv1);
        RT(tg); STAMPV(8, 8, tg); STAMPV(9, 8, tg);
    }
    BAR_RAW();
    LN_READ(0, smem + 2 * C::SLOT, 0);
#pragma unroll 1
    for (int s = 0; s < 9; ++s) {
        LAS char* sb = smem + ((s + 2) & 3) * C::SLOT;
        LAS char* nb = smem + ((s + 3) & 3) * C::SLOT;
        SBAR();
        LN_STEP(1, sb, 1, 0);
        LN_STEP(0, sb, 2, 1);
        LN_STEP(1, sb, 3, 0);
        BAR_RAW();
        SBAR();
        LN_STEP(0, nb, 0, 1);
    }
    LAS char* tab = smem + C::SLOT;
    {
        LAS char* sb = smem + 3 * C::SLOT;
        SBAR();
        LN_STEP(1, sb, 1, 0);
        LN_STEP(0, sb, 2, 1);
        LN_STEP(1, sb, 3, 0);
        if (!FIRST) BAR_LGKM();
        LN_MMA(1);
    }
    if (FIRST) {
        LAS float* raw = (LAS float*)(tab + T_RAW) + (wn * 2 + wm) * 96;
        if (lane < 16) { raw[lane] = sx[0]; raw[16 + lane] = sx[1]; raw[32 + lane] = sw_[0]; raw[48 + lane] = sw_[1]; }
        { const int ri = (r & 3) + 4 * (r >> 3); float dg = sxx[0];
#pragma unroll
          for (int i = 1; i < 16; ++i) dg = ri == i ? sxx[i] : dg;
          if (((r >> 2) & 1) == h) raw[64 + r] = dg; }
        asm volatile("s_waitcnt lgkmcnt(0)" ::: "memory");
        const float csum = raw[32 + r];
        const float mu = raw[r] * (1.f / DM), var = fmaxf(raw[64 + r] * (1.f / DM) - mu * mu, 0.f);
        const int tl = 64 * wm + 32 * wn + r, nl = 64 * wn + 32 * wm + r;
        if (h == 0) { *(LAS u32x4*)(tab + T_XC + tl * 16) = split3(-mu, true); *(LAS float*)(tab + T_RS + tl * 4) = __builtin_amdgcn_rsqf(var + LN_EPS); }
        else { *(LAS u32x4*)(tab + T_WC + nl * 16) = split3(csum, false); st32<MEGA>(ws + WS_CSQ + (bid * 128 + nl) * 4, csum); }
        BAR_LGKM();
    }
    {
        bf16x4 xc[2], wc[NB];
#pragma unroll
        for (int tb = 0; tb < 2; ++tb) xc[tb] = lds64(tab + T_XC + (64 * wm + 32 * (FIRST ? tb ^ wn : tb) + r) * 16 + h * 8);
#pragma unroll
        for (int j = 0; j < NB; ++j) wc[j] = lds64(tab + T_WC + ((NB * wn + (FIRST ? j ^ wm : j)) * 32 + r) * 16 + h * 8);
#pragma unroll
        for (int j = 0; j < NB; ++j)
#pragma unroll
            for (int tb = 0; tb < 2; ++tb) acc[j][tb] = SWAP ? MFMA32K8(wc[j], xc[tb], acc[j][tb]) : MFMA32K8(xc[tb], wc[j], acc[j][tb]);
    }
#undef LN_STEP
#undef LN_READ
#undef LN_MMA
}

struct Wts { const char *wo, *wg, *wu, *wd; };
constexpr int QKV_BASE = 32768, RS_SLOT = 18432;
DI void ln_tile(int mode, int bid, int& tm, int& tn) {
    const int xcd = bid & 7, bi = bid >> 3, pr = xcd >> 1, e = xcd & 1;
    if (mode == 0) { const int lin = e * 30 + bi; tm = 4 * pr + (lin & 3); tn = lin >> 2; }
    else { tm = 4 * pr + (bi & 3); tn = 8 * e + (bi >> 2); }
}
DI unsigned wqkv_off(int piece, int tn, int lane) { const int k = piece * 4 + (lane >> 4), g = lane & 15; return (unsigned)(k * 3840 + tn * 256 + (((g >> 2) ^ (k & 3)) << 6) + (g & 3) * 16); }
template <int NW>
DI void prefetch_res(unsigned sm0, const char* wtile, int slot0, int j, unsigned l16) {
#pragma unroll
    for (int i = 0; i < (20 + NW - 1) / NW; ++i) { const int pc = j + NW * i, st = pc >= 10;
        if (pc < 20) glds16s<true>(wtile + pc * 1024, l16, sm0 + (slot0 + st) * RS_SLOT + 8192 + (pc - 10 * st) * 1024); }
}

template <int MODE, bool FIRST, bool MEGA>
DI void phase_ln(LAS char* smem, const char* X, const char* W0, const char* stats, char* out0, char* out1, char* ws, const Wts& wt,
                 int cin, int cout, unsigned& tag, int wave DBG_ARG) {
    using C = LnCfg<MODE>;
    PHASE_IDS();
    const unsigned sm0 = (unsigned)(size_t)smem;
    int tm, tn;
    ln_tile(MODE, bid, tm, tn);
    LAS char* tab = smem + C::SLOT;

    if (MODE == 0 && bi >= 30) {
        if (FIRST) {
            const int blk = (xcd * 2 + bi - 30) * 3 + (tid >> 8), m = blk / 24, c = tid & 7, rg = (tid & 255) >> 3;
            const char* p = (m ? wt.wu : wt.wg) + rg * 3072 + (blk - 24 * m) * 128 + c * 16;
            float s[8];
#pragma unroll
            for (int i = 0; i < 8; ++i) s[i] = 0.f;
            u32x4 v[20];
#pragma unroll
            for (int j = 0; j < 20; ++j) v[j] = *(const u32x4*)(p + j * 32 * 3072);
#pragma unroll
            for (int j = 0; j < 20; ++j)
#pragma unroll
                for (int i = 0; i < 4; ++i) { s[2 * i] += bflo(v[j][i]); s[2 * i + 1] += bfhi(v[j][i]); }
            LAS float* red = (LAS float*)smem;
#pragma unroll
            for (int i = 0; i < 8; ++i) red[rg * 192 + (tid >> 8) * 64 + c * 8 + i] = s[i];
            BAR_LGKM();
            if (tid < 192) {
                float t = 0.f;
#pragma unroll
                for (int i = 0; i < 32; ++i) t += red[i * 192 + tid];
                const int b2 = (xcd * 2 + bi - 30) * 3 + (tid >> 6), m2 = b2 / 24, f = (b2 - 24 * m2) * 64 + (tid & 63), tf = f / 96, fl = f - tf * 96;
                st32<MEGA>(ws + WS_CSGU + (tf * 192 + (fl >> 4) * 32 + m2 * 16 + (fl & 15)) * 4, t);
            }
            if (MEGA) { tag = get_tag(ws); WAITV(0); }
        }
        return;
    }

    if (w < 8) {
        const int j = w;
        unsigned off[C::PPW];
#pragma unroll
        for (int p = 0; p < 2; ++p) {
            if (FIRST) { const int row = (j + 8 * p) * 8 + (lane >> 3); off[p] = (unsigned)((tm * 128 + row) * 1280 + (((lane & 7) ^ ((row >> 1) & 7)) << 4)); }
            else off[p] = (unsigned)((2 * tm + p) * 81920 + j * 1024 + lane * 16);
        }
#pragma unroll
        for (int p = 2; p < C::PPW; ++p) {
            const int piece = j + 8 * (p - 2);
            if (MODE == 0) off[p] = wqkv_off(piece, tn, lane);
            else off[p] = (unsigned)(tn * 245760 + piece * 1024 + lane * 16);
        }
        constexpr int XSTAGE = FIRST ? 128 : 8192, WSTAGE = MODE == 0 ? 64 * 3840 : 24576;
        int kb = FIRST ? (3 * (tm & 3) + 7 * tn) % 10 : 0;
        constexpr bool WDEV = MEGA && MODE == 1;
        auto issue = [&](int s) {
            const unsigned sb = sm0 + ((s + 2) & 3) * C::SLOT + j * 1024;
#pragma unroll
            for (int p = 0; p < 2; ++p) glds16s<MEGA && !FIRST>(X + kb * XSTAGE, off[p], sb + p * 8192);
#pragma unroll
            for (int p = 2; p < C::PPW; ++p) glds16s<WDEV>(W0 + kb * WSTAGE, off[p], sb + p * 8192);
            kb = (kb == 9) ? 0 : kb + 1;
        };
        if (FIRST) {
            issue(0); issue(1);
            WAITV(C::PPW);
            BAR_RAW();
#pragma unroll 1
            for (int s = 0; s < 8; ++s) { issue(s + 2); WAITV(C::PPW); BAR_RAW(); }
            WAITV(0);
            BAR_RAW();
            if (MEGA) tag = get_tag(ws);
            BAR_RAW();
        } else {
            if (MEGA) {
#pragma unroll
                for (int p = 0; p < C::PPW; ++p) PIN(off[p]);
                BAR_RAW();
#pragma unroll
                for (int s = 0; s < 2; ++s)
#pragma unroll
                    for (int p = 0; p < 2; ++p) glds16s<true>(X + s * XSTAGE, off[p], sm0 + (s + 2) * C::SLOT + j * 1024 + p * 8192);
                kb = 2;
            } else { issue(0); issue(1); }
            asm volatile("" ::: "memory");
            const char* sp = stats + (2 * tm + (tid >> 8)) * 4096 + (tid & 3) * 1024 + ((tid >> 2) & 63) * 8;
            const f32x2 st0 = __builtin_bit_cast(f32x2, ld64<MEGA>(sp)), st1 = __builtin_bit_cast(f32x2, ld64<MEGA>(sp + 512));
            const float cs = ld32<MEGA>(ws + (MODE == 0 ? WS_CSQ + bid * 512 : WS_CSGU + tn * 768) + (tid < C::NCS ? tid : 0) * 4);
            WAITV((MEGA ? 2 : C::PPW) + 3);
            BAR_RAW();
#pragma unroll 1
            for (int s = 0; s < 8; ++s) { issue(s + 2); WAITV(C::PPW); BAR_RAW(); }
            if (MEGA && MODE == 1) {
                prefetch_res<8>(sm0, ws + WS_WD + (4 * e + (bi >> 3)) * 245760, 0, j, lane * 16);
                if (j < 4) WAITV(3); else WAITV(2);
            } else WAITV(0);
            BAR_RAW();
            asm volatile("" ::"v"(cs));
            const float mu = quad_sum(st0[0] + st1[0]) * 0.125f, d0 = st0[0] - mu, d1 = st1[0] - mu;
            const float m2 = quad_sum(st0[1] + st1[1] + 80.f * (d0 * d0 + d1 * d1));
            if ((tid & 3) == 0) { *(LAS u32x4*)(tab + T_XC + (tid >> 2) * 16) = split3(-mu, true);
                                  *(LAS float*)(tab + T_RS + (tid >> 2) * 4) = __builtin_amdgcn_rsqf(m2 * (1.f / DM) + LN_EPS); }
            if (tid < C::NCS) *(LAS u32x4*)(tab + T_WC + tid * 16) = split3(cs, false);
            BAR_LGKM();
        }
        return;
    }

    const int wm = w & 1, wn = (w >> 1) & 1, r = lane & 31, h = lane >> 5;
    LAS const float* trs = (LAS const float*)(tab + T_RS);
    if (MEGA && FIRST) tag = get_tag(ws);
    unsigned long long tb0 = 0;
    f32x16 acc[C::NB][2];
#pragma unroll
    for (int j = 0; j < C::NB; ++j)
#pragma unroll
        for (int tb = 0; tb < 2; ++tb)
#pragma unroll
            for (int i = 0; i < 16; ++i) acc[j][tb][i] = 0.f;

    if (MODE == 1) {
        STAMPW(1, 8); ln_mainloop<MODE, FIRST, true, MEGA>(smem, acc, wm, wn, lane, ws, tm, bid, cin, tag, tb0 DBG_PASS(kid)); STAMPW(2, 8); STAMPV(11, 8, tb0);
        char* hp = out0 + (unsigned)(((2 * tm + wm) * 192 + tn * 12 + 6 * wn + h) * 1024 + r * 16);
#pragma unroll
        for (int tb = 0; tb < 2; ++tb) {
            const float rs = trs[64 * wm + 32 * tb + r], rs2 = rs * rs;
#pragma unroll
            for (int j = 0; j < 3; ++j) {
                unsigned pk[2][2];
#pragma unroll
                for (int qd = 0; qd < 2; ++qd) {
                    float hv[4];
#pragma unroll
                    for (int i = 0; i < 4; ++i) hv[i] = fmaxf(acc[j][tb][4 * qd + i] * rs2, 0.f) * acc[j][tb][8 + 4 * qd + i];
                    pk[qd][0] = pack2(hv[0], hv[1]); pk[qd][1] = pack2(hv[2], hv[3]);
                }
                auto s0 = __builtin_amdgcn_permlane32_swap(pk[0][0], pk[1][0], false, false);
                auto s1 = __builtin_amdgcn_permlane32_swap(pk[0][1], pk[1][1], false, false);
                st128<MEGA>(hp + j * 2048 + tb * 512, (u32x4){s0[0], s1[0], s0[1], s1[1]});
                SBAR();
            }
        }
    } else if (tn < 10) {
        STAMPW(1, 8); ln_mainloop<MODE, FIRST, true, MEGA>(smem, acc, wm, wn, lane, ws, tm, bid, cin, tag, tb0 DBG_PASS(kid)); STAMPW(2, 8); STAMPV(11, 8, tb0);
        const float sc = tn < 5 ? QSCALE : 1.f;
        char* qp = out0 + (unsigned)(((2 * tm + wm) * 160 + tn * 16 + 8 * wn + h) * 1024 + r * 16);
#pragma unroll
        for (int tb = 0; tb < 2; ++tb) {
            const int tba = FIRST ? tb ^ wn : tb;
            const float rs = trs[64 * wm + 32 * tba + r] * sc;
#pragma unroll
            for (int cb = 0; cb < 2; ++cb)
#pragma unroll
                for (int qp2 = 0; qp2 < 2; ++qp2) {
                    const f32x16& a = acc[cb][tb];
                    auto s0 = __builtin_amdgcn_permlane32_swap(pack2(a[8 * qp2] * rs, a[8 * qp2 + 1] * rs), pack2(a[8 * qp2 + 4] * rs, a[8 * qp2 + 5] * rs), false, false);
                    auto s1 = __builtin_amdgcn_permlane32_swap(pack2(a[8 * qp2 + 2] * rs, a[8 * qp2 + 3] * rs), pack2(a[8 * qp2 + 6] * rs, a[8 * qp2 + 7] * rs), false, false);
                    st128<MEGA>(qp + (4 * (FIRST ? cb ^ wm : cb) + 2 * qp2) * 1024 + tba * 512, (u32x4){s0[0], s1[0], s0[1], s1[1]});
                    SBAR();
                }
        }
    } else {
        STAMPW(1, 8); ln_mainloop<MODE, FIRST, false, MEGA>(smem, acc, wm, wn, lane, ws, tm, bid, cin, tag, tb0 DBG_PASS(kid)); STAMPW(2, 8); STAMPV(11, 8, tb0);
        char* vp = out1 + (unsigned)((((tm * 8 + 4 * wm) * 2 + h) * 640 + (tn - 10) * 128 + 64 * wn + r) * 16);
#pragma unroll
        for (int tb = 0; tb < 2; ++tb)
#pragma unroll
            for (int s = 0; s < 2; ++s) {
                const int tba = FIRST ? tb ^ wn : tb, tl = 64 * wm + 32 * tba + 16 * s + 4 * h;
                const f32x4 r0 = *(LAS const f32x4*)(trs + tl), r1 = *(LAS const f32x4*)(trs + tl + 8);
#pragma unroll
                for (int cb = 0; cb < 2; ++cb) {
                    const f32x16& a = acc[cb][tb];
                    st128<MEGA>(vp + (2 * tba + s) * 20480 + (FIRST ? cb ^ wm : cb) * 512,
                          (u32x4){pack2(a[8 * s] * r0[0], a[8 * s + 1] * r0[1]), pack2(a[8 * s + 2] * r0[2], a[8 * s + 3] * r0[3]),
                                  pack2(a[8 * s + 4] * r1[0], a[8 * s + 5] * r1[1]), pack2(a[8 * s + 6] * r1[2], a[8 * s + 7] * r1[3])});
                    SBAR();
                }
            }
    }
    RT(ts);
    if (MEGA) signal(ws, tag, cout + (MODE == 1 ? ((2 * tm + wm) * 16 + tn) * 2 + wn : (tn * 16 + tm) * 4 + 2 * wm + wn), lane);
    STAMPV(10, 8, ts);
}

template <int KT, bool MEGA>
DI void phase_res(LAS char* smem, const char* A, const char* W, const char* res, char* out, char* stats, char* ws, bool resrow, bool outrow,
                  u32x2 (&rv)[5], int cin, int cout, bool wready, const char* wnext, unsigned tag, int wave DBG_ARG) {
    constexpr int NS = KT / 64, NSLOT = 5, SLOT = RS_SLOT, DEP = 3, SL0 = KT == DM ? 3 : 0;
    constexpr bool TAILS = KT == DM;
    PHASE_IDS();
    const int tm = 8 * pr + (bi & 7), tn = 4 * e + (bi >> 3);

    if (w < 8) {
        const int j = w;
        const unsigned off = (unsigned)(j * 1024 + lane * 16);
        const char* ab = A + tm * (KT * 128);
        const char* wb = W + tn * (NS * 10240);
        const unsigned sm0 = (unsigned)(size_t)smem;
        unsigned sb = sm0 + SL0 * SLOT + j * 1024;
        int sl = SL0, ntm, ntn;
        ln_tile(TAILS ? 1 : 0, bid, ntm, ntn);
        const unsigned qo0 = wqkv_off(j, ntn, lane), qo1 = wqkv_off(j + 8, ntn, lane);
        auto issue = [&]() {
            glds16s<MEGA>(ab, off, sb);
            glds16s<MEGA>(wb, off, sb + 8192);
            if (j < 2) glds16s<MEGA>(wb + 8192, off, sb + 16384);
            ab += 8192; wb += 10240;
            if (sl == NSLOT - 1) { sl = 0; sb -= (NSLOT - 1) * SLOT; } else { ++sl; sb += SLOT; }
        };
#define RS_WAIT(n, x) do { if (j < 2) WAITV(3 * (n) + (x)); else WAITV(2 * (n) + (x)); } while (0)
        if (MEGA) BAR_RAW();
        if (MEGA && wready) {
            glds16s<true>(ab, off, sb); glds16s<true>(ab + 8192, off, sb + SLOT);
            ab += 2 * 8192; wb += 2 * 10240; sl = (SL0 + 2) % NSLOT; sb = sm0 + sl * SLOT + j * 1024;
            issue();
            RS_WAIT(1, 1);
        } else {
            issue(); issue(); issue();
            RS_WAIT(2, 0);
        }
        BAR_RAW();
#pragma unroll 1
        for (int s = 0; s < NS - DEP; ++s) { issue(); RS_WAIT(2, 0); BAR_RAW(); }
        if (MEGA && TAILS) {
#pragma unroll
            for (int s = 0; s < 2; ++s)
#pragma unroll
                for (int p = 0; p < 3; ++p) glds16s<true>(wnext + ntn * 245760 + s * 24576 + p * 8192, off, sm0 + (s + 2) * 40960 + 16384 + p * 8192 + j * 1024);
            RS_WAIT(1, 6); BAR_RAW(); WAITV(6); BAR_RAW();
        } else if (MEGA && wnext && bi < 30) {
#pragma unroll
            for (int s = 0; s < 2; ++s) {
                glds16s<false>(wnext + s * (64 * 3840), qo0, sm0 + QKV_BASE + (s + 2) * 32768 + 16384 + j * 1024);
                glds16s<false>(wnext + s * (64 * 3840), qo1, sm0 + QKV_BASE + (s + 2) * 32768 + 24576 + j * 1024);
            }
            RS_WAIT(1, 4); BAR_RAW(); WAITV(4); BAR_RAW();
        } else { RS_WAIT(1, 0); BAR_RAW(); WAITV(0); BAR_RAW(); }
#undef RS_WAIT
        return;
    }

    const int cw = w - 8, r = lane & 15, g = lane >> 4, row = 16 * cw + r;
    if (resrow) {
#pragma unroll
        for (int j = 0; j < 5; ++j) rv[j] = *(const u32x2*)(res + (unsigned)((tm * 64 + row) * 1280 + tn * 160 + j * 32 + g * 8));
    } else if (!MEGA) {
#pragma unroll
        for (int j = 0; j < 5; ++j) rv[j] = *(const u32x2*)(res + (unsigned)((tm * 80 + tn * 10 + 2 * j + (g >> 1)) * 1024 + row * 16 + (g & 1) * 8));
    }
    const unsigned xa = g * 1024 + row * 16, xt = g * 1024 + row * 8, wa = 8192 + lane * 16;
    f32x4 acc[5];
#pragma unroll
    for (int j = 0; j < 5; ++j) acc[j] = (f32x4){0.f, 0.f, 0.f, 0.f};
    bf16x8 xf[2], wf[2][5];
    if (MEGA) {
        unsigned pv0, pv1;
        await_bar(w == 8, ws, tag, cin + (TAILS ? tm * 64 + lane : tm * 32 + (lane & 31)), pv0, pv1);
        WAITV(0); POLL_DONE(pv0, pv1);
        RT(tg); STAMPV(8, 8, tg); STAMPV(9, 8, tg);
    }
    BAR_RAW();
    STAMPW(1, 8);
    { RT(tb0); STAMPV(11, 8, tb0); }
#pragma unroll
    for (int ks = 0; ks < 2; ++ks) {
        xf[ks] = lds128(smem + SL0 * SLOT + xa + ks * 4096);
#pragma unroll
        for (int j = 0; j < 5; ++j) wf[ks][j] = lds128(smem + SL0 * SLOT + wa + ks * 5120 + j * 1024);
    }
    int slot = SL0;
#define RS_STAGE(TAIL) do { \
        slot = (slot + 1 == NSLOT) ? 0 : slot + 1; \
        LAS char* nb = smem + slot * SLOT; \
        BAR_RAW();                                   \
        SBAR(); \
        _Pragma("unroll") for (int ks = 0; ks < 2; ++ks) { \
            _Pragma("unroll") for (int j = 0; j < 5; ++j) { \
                acc[j] = MFMA16(wf[ks][j], xf[ks], acc[j]); \
                wf[ks][j] = lds128(nb + wa + ks * 5120 + j * 1024); \
                if (j == 4) xf[ks] = (TAIL) ? cat(lds64(nb + xt + ks * 4096), lds64(nb + xt + ks * 4096 + 512)) : lds128(nb + xa + ks * 4096); \
                SBAR(); \
            } } } while (0)
#pragma unroll 1
    for (int s = 0; s < NS - 1 - (TAILS ? 2 : 0); ++s) RS_STAGE(false);
    if (TAILS) {
#pragma unroll 1
        for (int s = 0; s < 2; ++s) RS_STAGE(true);
    }
#undef RS_STAGE
#pragma unroll
    for (int ks = 0; ks < 2; ++ks)
#pragma unroll
        for (int j = 0; j < 5; ++j) acc[j] = MFMA16(wf[ks][j], xf[ks], acc[j]);
    STAMPW(2, 8);
#pragma unroll
    for (int j = 0; j < 5; ++j)
        rv[j] = (u32x2){pack2(acc[j][0] + bflo(rv[j][0]), acc[j][1] + bfhi(rv[j][0])), pack2(acc[j][2] + bflo(rv[j][1]), acc[j][3] + bfhi(rv[j][1]))};
    if (outrow) {
#pragma unroll
        for (int j = 0; j < 5; ++j) st64<false>(out + (unsigned)((tm * 64 + row) * 1280 + tn * 160 + j * 32 + g * 8), rv[j]);
    } else {
        char* op = out + (unsigned)((tm * 80 + tn * 10) * 1024 + row * 16);
#pragma unroll
        for (int jp = 0; jp < 2; ++jp) {
            auto s0 = __builtin_amdgcn_permlane16_swap(rv[2 * jp][0], rv[2 * jp + 1][0], false, false);
            auto s1 = __builtin_amdgcn_permlane16_swap(rv[2 * jp][1], rv[2 * jp + 1][1], false, false);
            st128<MEGA>(op + (4 * jp + 2 * (g & 1) + (g >> 1)) * 1024, (u32x4){s0[0], s1[0], s0[1], s1[1]});
        }
        st64<MEGA>(op + (8 + (g >> 1)) * 1024 + (g & 1) * 8, rv[4]);
        float v[20]; float sum = 0.f;
#pragma unroll
        for (int j = 0; j < 5; ++j) {
            v[4 * j] = bflo(rv[j][0]); v[4 * j + 1] = bfhi(rv[j][0]); v[4 * j + 2] = bflo(rv[j][1]); v[4 * j + 3] = bfhi(rv[j][1]);
            sum += (v[4 * j] + v[4 * j + 1]) + (v[4 * j + 2] + v[4 * j + 3]);
        }
        const float mean = xsum16_32(sum) * (1.f / 80.f);
        float m2 = 0.f;
#pragma unroll
        for (int i = 0; i < 20; ++i) { const float d = v[i] - mean; m2 += d * d; }
        m2 = xsum16_32(m2);
        if (g == 0) st64<MEGA>(stats + (unsigned)((tm * 8 + tn) * 512 + row * 8), (u32x2){__float_as_uint(mean), __float_as_uint(m2)});
        RT(ts);
        if (MEGA) signal(ws, tag, cout + (tm * 8 + tn) * 4 + cw, lane);
        STAMPV(10, 8, ts);
    }
}

constexpr int ACH = 23552, A_V = 12288, VSTEP = 704, VHALF = 352, ANSL = 6, A_Q = ANSL * ACH;
DI float swap_max(float m) { auto rr = __builtin_amdgcn_permlane32_swap(__float_as_uint(m), __float_as_uint(m), false, false); return fmaxf(__uint_as_float(rr[0]), __uint_as_float(rr[1])); }
DI float max16(const f32x16& s) {
    float a = fmaxf(fmaxf(s[0], s[1]), s[2]), b = fmaxf(fmaxf(s[3], s[4]), s[5]);
    a = fmaxf(fmaxf(a, s[6]), s[7]); b = fmaxf(fmaxf(b, s[8]), s[9]);
    a = fmaxf(fmaxf(a, s[10]), s[11]); b = fmaxf(fmaxf(b, s[12]), s[13]);
    a = fmaxf(fmaxf(a, s[14]), s[15]);
    return swap_max(fmaxf(a, b));
}
DI bf16x8 packp(const f32x16& p, int s) {
    u32x4 u = {pack2(p[8 * s], p[8 * s + 1]), pack2(p[8 * s + 2], p[8 * s + 3]), pack2(p[8 * s + 4], p[8 * s + 5]), pack2(p[8 * s + 6], p[8 * s + 7])};
    return __builtin_bit_cast(bf16x8, u);
}

DI void retile_slab(int task, LAS char* scr, const Wts& wt, char* ws, int lane) {
    const unsigned sl = (unsigned)(size_t)scr, l16 = lane * 16;
    const int q = (lane & 15) >> 2, pp = lane & 3;
    if (task < 544) {
        const bool iso = task < 160;
        const int t2 = iso ? task : task - 160, nk = iso ? 20 : 48, ct = t2 / nk, ks = t2 - ct * nk;
#pragma unroll
        for (int p = 0; p < 5; ++p) { const int G = p * 64 + lane, kr = G / 10, c = G - kr * 10, k = ks * 32 + kr;
            const int row = !iso ? k : (k < 512 ? 20 * (k >> 4) + (k & 15) : 20 * ((k - 512) >> 2) + 16 + (k & 3));
            glds16s<false>(iso ? wt.wo : wt.wd, (unsigned)(row * 1280 + ct * 160 + c * 16), sl + p * 1024); }
        WAITV(0);
        LAS const char* rp = scr + (8 * (lane >> 4) + q) * 160 + pp * 8;
#pragma unroll
        for (int i = 0; i < 5; ++i) st128s(ws + (iso ? WS_WO : WS_WD) + t2 * 5120 + i * 1024, l16, __builtin_bit_cast(u32x4, cat(ldstr(rp + i * 32), ldstr(rp + 640 + i * 32))));
    } else if (task < 1184) {
        const int t2 = task - 544, tf = t2 / 40, ks = t2 - tf * 40;
#pragma unroll
        for (int p = 0; p < 3; ++p) { const int G = p * 64 + lane, k = G / 12, c = G - k * 12; const unsigned so = (unsigned)((ks * 16 + k) * 3072 + tf * 192 + c * 16);
            glds16s<false>(wt.wg, so, sl + p * 1024); glds16s<false>(wt.wu, so, sl + 3072 + p * 1024); }
        WAITV(0);
        LAS const char* rp = scr + ((lane >> 4) & 1) * 3072 + (8 * (lane >> 5) + q) * 192 + pp * 8;
#pragma unroll
        for (int i = 0; i < 6; ++i) st128s(ws + WS_WGU + t2 * 6144 + i * 1024, l16, __builtin_bit_cast(u32x4, cat(ldstr(rp + i * 32), ldstr(rp + 768 + i * 32))));
    }
}

template <bool MEGA>
DI void phase_attn(LAS char* smem, const char* QK, const char* V, char* O, char* ws, const Wts& wt, bool first, int cin, int cout, unsigned tag, int wave DBG_ARG) {
    PHASE_IDS();
    const int head = 16 * e + (bi >> 1), qt = 2 * pr + (bi & 1), par = head & 1;
    const unsigned sm0 = (unsigned)(size_t)smem, l16 = lane * 16;
    const int g0 = 5 * (head >> 1) + 3 * par, gt = 5 * (head >> 1) + 2;
    auto kq_src = [&](int sec, int blk256, int i) { const int grp = i / 3, part = i - 3 * grp; return QK + ((blk256 * 4 + grp) * 160 + sec * 80 + (part == 2 ? gt : g0 + part)) * 1024; };
    auto v_off = [&](int i, bool& ok) { const int G = i * 64 + lane, sh = G / 22, row = G - sh * 22; ok = row < 20; return (unsigned)((sh * 640 + head * 20 + row) * 16); };
    const int c0 = 2 * pr;
    {
        bool ok; unsigned vo = v_off(w, ok), pv0 = 0, pv1 = 0;
        unsigned long long ksrc = sgpr64(kq_src(1, c0, w)), qsrc = sgpr64(kq_src(0, qt, w)), vsrc = sgpr64(V + c0 * 327680);
        if (MEGA) {
            const int sec = min(lane >> 4, 2), ct = (head * 20 + ((lane >> 3) & 1) * 19) >> 7;
            PIN(vo); asm volatile("" : "+s"(ksrc), "+s"(qsrc), "+s"(vsrc));
            await_bar(w == 8, ws, tag, cin + ((5 * sec + ct) * 16 + 2 * (sec ? c0 : qt)) * 4 + (lane & 7), pv0, pv1);
            RT(tg); STAMPV(8, 8, tg); STAMPV(9, 8, tg);
        }
        glds16s<MEGA>((const char*)ksrc, l16, sm0 + w * 1024);
        glds16s<MEGA>((const char*)qsrc, l16, sm0 + A_Q + w * 1024);
        if (w < 11 && ok) glds16s<MEGA>((const char*)vsrc, vo, sm0 + A_V + w * 1024);
        if (MEGA) { WAITV(3); POLL_DONE(pv0, pv1); }
        if (tid < ANSL * 64) {
            const unsigned cv = (tid & 1) ? 0u : 0x3F803F80u;
            *(LAS u32x4*)(smem + (tid >> 6) * ACH + A_V + ((tid & 63) >> 1) * VHALF + 320 + (tid & 1) * 16) = (u32x4){cv, cv, cv, cv};
        }
    }
    if (w >= 8) {
        const int lw = w - 8;
        __builtin_amdgcn_s_setprio(3);
        unsigned vo[3]; bool ok[3];
#pragma unroll
        for (int i = 0; i < 3; ++i) vo[i] = v_off(lw + 4 * i, ok[i]);
        auto chunk = [&](int c) {
            const int cc = (c + c0) & 7; const unsigned sb = sm0 + (c >= ANSL ? c - ANSL : c) * ACH;
#pragma unroll
            for (int i = 0; i < 3; ++i) glds16s<MEGA>(kq_src(1, cc, lw + 4 * i), l16, sb + (lw + 4 * i) * 1024);
#pragma unroll
            for (int i = 0; i < 3; ++i) if ((i < 2 || lw < 3) && ok[i]) glds16s<MEGA>(V + cc * 327680, vo[i], sb + A_V + (lw + 4 * i) * 1024);
        };
        asm volatile("s_waitcnt vmcnt(0) lgkmcnt(0)\n\ts_barrier" ::: "memory");
        const int kvl = cin + (5 + (lane >> 5) * 5 + ((head * 20 + ((lane >> 4) & 1) * 19) >> 7)) * 64;
        if (MEGA) await<4>(ws, tag, kvl + (2 * c0 + 2 + (lane & 1)) * 4);
        chunk(1);
        if (MEGA) await<4>(ws, tag, kvl + (lane & 15) * 4);
        chunk(2);
        WAITV(0); BAR_RAW();
        chunk(3); chunk(4); chunk(5);
        if (first && lw < 3) retile_slab(bid + 256 * lw, smem + A_Q + lw * 6144, wt, ws, lane);
        WAITV(0); BAR_RAW();
        chunk(6); chunk(7);
        const int tno = 4 * e + (bi >> 3);
        if (first) {
            if (lw < 2) retile_slab(bid + 256 * (lw + 3), smem + A_Q + lw * 6144, wt, ws, lane);
            if (MEGA) signal(ws, tag, CN_R + bid * 4 + lw, lane); else WAITV(0);
            BAR_RAW();
            if (MEGA) { await<4>(ws, tag, CN_R + (tno * 20 + (lane & 3)) * 4); await<4>(ws, tag, CN_R + (lw * 64 + lane) * 4); }
        } else {
            WAITV(0);
            const int t = tid - 512;
            unsigned tv = *(const unsigned*)(ws + WS_WO + e * 409600 + bi * 12800 + min(t, 99) * 128);
            tv ^= *(const unsigned*)(ws + WS_WGU + e * 1966080 + bi * 61440 + t * 128);
            tv ^= *(const unsigned*)(ws + WS_WGU + e * 1966080 + bi * 61440 + min(256 + t, 479) * 128);
            BAR_RAW();
            asm volatile("" ::"v"(tv));
        }
        if (MEGA) { prefetch_res<4>(sm0, ws + WS_WO + tno * 102400, 3, lw, l16); WAITV(0); }
        __builtin_amdgcn_s_setprio(0);
        if (MEGA) BAR_RAW();
        return;
    }

    const int r = lane & 31, h = lane >> 5;
    const unsigned k0a = h * 1024 + r * 16, k1a = 2048 + r * 16 + par * 8, va = A_V + h * VHALF + min(r, 21) * 16;
    f32x16 zero, negm, o;
#pragma unroll
    for (int i = 0; i < 16; ++i) { zero[i] = 0.f; o[i] = 0.f; }
    bf16x8 v1 = {0, 0, 0, 0, 0, 0, 0, 0};
    u32x4 pw0 = {0, 0, 0, 0}, pw1 = {0, 0, 0, 0};
    float e0 = 0.f, e1 = 0.f, e2 = 0.f, e3 = 0.f;
    PIN(o); PIN(v1); PIN(pw0); PIN(pw1);
    asm volatile("s_waitcnt vmcnt(0) lgkmcnt(0)\n\ts_barrier" ::: "memory");
    STAMPW(1, 0);
    { RT(tb0); STAMPV(11, 0, tb0); }
    bf16x8 q0; bf16x4 q1;
    { LAS const char* qp = smem + A_Q + (w >> 1) * 3072 + (w & 1) * 512;
      q0 = lds128(qp + k0a); q1 = lds64(qp + k1a);
      if (h) q1 = (bf16x4){0, 0, 0, 0}; }
    LAS const char* pk0 = smem + k0a;
    LAS const char* pk1 = smem + k1a;
    LAS const char* pv = smem + va;
    f32x16 sA, sB;
    {
        const bf16x8 a = lds128(pk0); const bf16x4 b = lds64(pk1);
        f32x16 s = MFMA32(a, q0, zero);
        s = MFMA32K8(b, q1, s);
        const float m = max16(s);
#pragma unroll
        for (int i = 0; i < 16; ++i) negm[i] = -m;
        sA = MFMA32(a, q0, negm);
        sA = MFMA32K8(b, q1, sA);
    }
    bf16x8 ka = lds128(pk0 + 512), v0 = lds128(pv);
    bf16x4 kb = lds64(pk1 + 512);
#define EX(x) __builtin_amdgcn_exp2f(x)
#define ATT_STEP(SN, SC, PK0, PK1, PV0, PV1) do { \
        float t0, t1, t2, t3; \
        SN = MFMA32(ka, q0, negm); PIN(SN); ka = lds128(PK0); \
        pw1[2] = pack2(e0, e1); pw1[3] = pack2(e2, e3); PIN(pw1); SBAR(); \
        t0 = EX(SC[0]); t1 = EX(SC[1]); t2 = EX(SC[2]); t3 = EX(SC[3]); PIN(t0); PIN(t1); PIN(t2); PIN(t3); SBAR(); \
        o = MFMA32(v1, __builtin_bit_cast(bf16x8, pw1), o); PIN(o); v1 = lds128(PV1); \
        pw0[0] = pack2(t0, t1); pw0[1] = pack2(t2, t3); PIN(pw0); SBAR(); \
        t0 = EX(SC[4]); t1 = EX(SC[5]); t2 = EX(SC[6]); t3 = EX(SC[7]); PIN(t0); PIN(t1); PIN(t2); PIN(t3); SBAR(); \
        SN = MFMA32K8(kb, q1, SN); PIN(SN); kb = lds64(PK1); \
        pw0[2] = pack2(t0, t1); pw0[3] = pack2(t2, t3); PIN(pw0); SBAR(); \
        t0 = EX(SC[8]); t1 = EX(SC[9]); t2 = EX(SC[10]); t3 = EX(SC[11]); PIN(t0); PIN(t1); PIN(t2); PIN(t3); SBAR(); \
        o = MFMA32(v0, __builtin_bit_cast(bf16x8, pw0), o); PIN(o); v0 = lds128(PV0); \
        pw1[0] = pack2(t0, t1); pw1[1] = pack2(t2, t3); PIN(pw1); SBAR(); \
        e0 = EX(SC[12]); e1 = EX(SC[13]); e2 = EX(SC[14]); e3 = EX(SC[15]); PIN(e0); PIN(e1); PIN(e2); PIN(e3); SBAR(); \
    } while (0)
#define KOF(u) (((u) >> 1) * 3072 + ((u) & 1) * 512)
#pragma unroll 1
    for (int c = 0; c < 8; ++c) {
        const int step = (c == ANSL - 1) ? -(ANSL - 1) * ACH : ACH;
        LAS const char* nk0 = pk0 + step; LAS const char* nk1 = pk1 + step; LAS const char* nv = pv + step;
        if (c == 4) __builtin_amdgcn_s_setprio(2); else if (c == 0 || c == 2 || c == 5 || c == 7) __builtin_amdgcn_s_setprio(1);
        ATT_STEP(sB, sA, pk0 + KOF(2), pk1 + KOF(2), pv + 2 * VSTEP, pv + 1 * VSTEP);
        ATT_STEP(sA, sB, pk0 + KOF(3), pk1 + KOF(3), pv + 4 * VSTEP, pv + 3 * VSTEP);
        ATT_STEP(sB, sA, pk0 + KOF(4), pk1 + KOF(4), pv + 6 * VSTEP, pv + 5 * VSTEP);
        ATT_STEP(sA, sB, pk0 + KOF(5), pk1 + KOF(5), pv + 8 * VSTEP, pv + 7 * VSTEP);
        if (c == 1 || c == 6) __builtin_amdgcn_s_setprio(2); else if (c != 3 && c != 4) __builtin_amdgcn_s_setprio(0);
        ATT_STEP(sB, sA, pk0 + KOF(6), pk1 + KOF(6), pv + 10 * VSTEP, pv + 9 * VSTEP);
        ATT_STEP(sA, sB, pk0 + KOF(7), pk1 + KOF(7), pv + 12 * VSTEP, pv + 11 * VSTEP);
        if (c == 0 || c == 2 || c == 5) { BAR_RAW(); __builtin_amdgcn_s_setprio(3); }
        ATT_STEP(sB, sA, nk0, nk1, pv + 14 * VSTEP, pv + 13 * VSTEP);
        ATT_STEP(sA, sB, nk0 + KOF(1), nk1 + KOF(1), nv, pv + 15 * VSTEP);
        pk0 = nk0; pk1 = nk1; pv = nv;
    }
#undef KOF
#undef ATT_STEP
    pw1[2] = pack2(e0, e1); pw1[3] = pack2(e2, e3);
    o = MFMA32(v1, __builtin_bit_cast(bf16x8, pw1), o);
    STAMPW(2, 0);
    float l;
    { auto rr = __builtin_amdgcn_permlane32_swap(__float_as_uint(o[8]), __float_as_uint(o[8]), false, false); l = __uint_as_float(rr[1]); }
    float chk = 0.f;
#pragma unroll
    for (int i = 0; i < 12; ++i) chk += o[i] * 0.f;
    const bool okr = (chk == 0.f) && (l <= 1.8446744e19f);
    if (__builtin_expect(!__all(okr), 0)) {
        float m = -INFINITY;
#pragma unroll
        for (int i = 0; i < 16; ++i) o[i] = 0.f;
        const unsigned cv = r == 20 ? 0x3F803F80u : 0u;
#pragma unroll 1
        for (int kbk = 0; kbk < 64; ++kbk) {
            const char* kp = QK + ((kbk >> 1) * 160 + 80) * 1024 + ((kbk & 1) * 32 + r) * 16;
            const u32x2 ka0 = ld64<MEGA>(kp + (g0 + h) * 1024), ka1 = ld64<MEGA>(kp + (g0 + h) * 1024 + 8);
            f32x16 s = MFMA32(__builtin_bit_cast(bf16x8, (u32x4){ka0[0], ka0[1], ka1[0], ka1[1]}), q0, zero);
            s = MFMA32K8(__builtin_bit_cast(bf16x4, ld64<MEGA>(kp + gt * 1024 + par * 8)), q1, s);
            const float mn = fmaxf(m, max16(s));
            const float al = __builtin_amdgcn_exp2f(m - mn);
            m = mn;
#pragma unroll
            for (int i = 0; i < 16; ++i) { o[i] *= al; s[i] = __builtin_amdgcn_exp2f(s[i] - mn); }
#pragma unroll
            for (int st = 0; st < 2; ++st) {
                const char* vq = V + (((2 * kbk + st) * 2 + h) * 640 + head * 20 + min(r, 19)) * 16;
                const u32x2 va0 = ld64<MEGA>(vq), va1 = ld64<MEGA>(vq + 8);
                u32x4 vv = {va0[0], va0[1], va1[0], va1[1]};
                if (r >= 20) vv = (u32x4){cv, cv, cv, cv};
                o = MFMA32(__builtin_bit_cast(bf16x8, vv), packp(s, st), o);
            }
        }
        auto rr = __builtin_amdgcn_permlane32_swap(__float_as_uint(o[8]), __float_as_uint(o[8]), false, false); l = __uint_as_float(rr[1]);
    }
    {
        const float inv = 1.f / l;
        char* ob = O + (unsigned)((qt * 4 + (w >> 1)) * 81920);
        const int row = (w & 1) * 32 + r;
        auto s0 = __builtin_amdgcn_permlane32_swap(pack2(o[0] * inv, o[1] * inv), pack2(o[4] * inv, o[5] * inv), false, false);
        auto s1 = __builtin_amdgcn_permlane32_swap(pack2(o[2] * inv, o[3] * inv), pack2(o[6] * inv, o[7] * inv), false, false);
        st128<MEGA>(ob + (unsigned)((2 * head + h) * 1024 + row * 16), (u32x4){s0[0], s1[0], s0[1], s1[1]});
        if (h == 0) st64<MEGA>(ob + (unsigned)(65536 + head * 512 + row * 8), (u32x2){pack2(o[8] * inv, o[9] * inv), pack2(o[10] * inv, o[11] * inv)});
    }
    RT(ts);
    if (MEGA) { signal(ws, tag, cout + ((qt * 4 + (w >> 1)) * 32 + head) * 2 + (w & 1), lane); RT(td); STAMPV(12, 7, td); BAR_RAW(); }
    STAMPV(10, 0, ts); STAMPV(13, 7, ts);
#undef EX
}

#define SMEM_DECL(n) __shared__ __attribute__((aligned(1024))) char smem_[n]; LAS char* smem = (LAS char*)smem_
#define PH(k, wv, call) call
template <bool MEGA>
DI void run_phase(int ph, int layer, LAS char* smem, const char* x0, const char* wqkv, const Wts& wt, char* ws, char* out, u32x2 (&rv)[5], unsigned& tag, int wave) {
    if (ph == 0) PH(1, 8, (phase_ln<0, true, MEGA>(smem, x0, wqkv, nullptr, ws + WS_QK, ws + WS_V, ws, wt, 0, CN_Q, tag, wave DBG_PASS(1))));
    if (ph == 1) PH(6, 8, (phase_ln<0, false, MEGA>(smem + QKV_BASE, ws + WS_XB, wqkv, ws + WS_ST + SZ_ST, ws + WS_QK + 2 * SZ_X, ws + WS_V + SZ_X, ws, wt, CN_X + 1024, CN_Q + 1024, tag, wave DBG_PASS(6))));
    if (ph == 2) PH(2 + 5 * layer, 0, (phase_attn<MEGA>(smem, ws + WS_QK + layer * 2 * SZ_X, ws + WS_V + layer * SZ_X, ws + WS_O + layer * SZ_X, ws, wt, layer == 0,
                                       CN_Q + 1024 * layer, CN_O + 2048 * layer, tag, wave DBG_PASS(2 + 5 * layer))));
    if (ph == 3) PH(3 + 5 * layer, 8, (phase_res<DM, MEGA>(smem, ws + WS_O + layer * SZ_X, ws + WS_WO, layer ? ws + WS_XB : x0, ws + WS_XA + layer * SZ_X,
                                       ws + WS_ST + 2 * layer * SZ_ST, ws, layer == 0, false, rv, CN_O + 2048 * layer, CN_X + 2048 * layer, true, ws + WS_WGU, tag, wave DBG_PASS(3 + 5 * layer))));
    if (ph == 4) PH(4 + 5 * layer, 8, (phase_ln<1, false, MEGA>(smem, ws + WS_XA + layer * SZ_X, ws + WS_WGU, ws + WS_ST + 2 * layer * SZ_ST,
                                       ws + WS_H + layer * (SEQ * FF * 2), nullptr, ws, wt, CN_X + 2048 * layer, CN_H + 1024 * layer, tag, wave DBG_PASS(4 + 5 * layer))));
    if (ph == 5) PH(5 + 5 * layer, 8, (phase_res<FF, MEGA>(smem, ws + WS_H + layer * (SEQ * FF * 2), ws + WS_WD, ws + WS_XA + layer * SZ_X, layer ? out : ws + WS_XB,
                                       ws + WS_ST + SZ_ST, ws, false, layer != 0, rv, CN_H + 1024 * layer, CN_X + 1024, true, layer ? nullptr : wqkv, tag, wave DBG_PASS(5 + 5 * layer))));
}
__global__ __launch_bounds__(768) void k_mega(const char* x0, const char* wqkv, const char* wo, const char* wg, const char* wu, const char* wd,
                                              char* ws, char* out) {
    SMEM_DECL(LDS_BYTES);
    const Wts wt = {wo, wg, wu, wd};
    u32x2 rv[5];
#pragma unroll
    for (int j = 0; j < 5; ++j) rv[j] = (u32x2){0u, 0u};
    unsigned tag = 0;
    const int wave = __builtin_amdgcn_readfirstlane(threadIdx.x >> 6);
    if (wave == 11) {
        unsigned long long pc;
        asm volatile("s_getpc_b64 %0" : "=s"(pc));
        const int lane = threadIdx.x & 63;
        const GAS char* p = lane < 13 ? (const GAS char*)(pc & ~127ull) + min((int)(blockIdx.x >> 3) * 13 + lane, CODE_LINES - 1) * 128
                                      : (lane < 40 ? (const GAS char*)ws + (size_t)min(lane - 13, (int)(WS_CNT >> 21)) * (2u << 20)
                                                   : (const GAS char*)(lane < 42 ? out + (lane - 40) * (2u << 20) : (lane == 42 ? wo : (lane == 43 ? wd : (lane == 44 ? wg : wu)))));
        unsigned tv = *(const GAS unsigned*)p;
        asm volatile("" ::"v"(tv));
    }
    run_phase<true>(0, 0, smem, x0, wqkv, wt, ws, out, rv, tag, wave);
#pragma unroll 1
    for (int layer = 0; layer < 2; ++layer) {
        if (layer) run_phase<true>(1, layer, smem, x0, wqkv, wt, ws, out, rv, tag, wave);
        run_phase<true>(2, layer, smem, x0, wqkv, wt, ws, out, rv, tag, wave);
        run_phase<true>(3, layer, smem, x0, wqkv, wt, ws, out, rv, tag, wave);
        run_phase<true>(4, layer, smem, x0, wqkv, wt, ws, out, rv, tag, wave);
        run_phase<true>(5, layer, smem, x0, wqkv, wt, ws, out, rv, tag, wave);
    }
    if (blockIdx.x == 0 && wave == 8) __hip_atomic_store((GAS unsigned*)(ws + WS_EPOCH), tag, __ATOMIC_RELAXED, __HIP_MEMORY_SCOPE_AGENT);
}

extern "C" void kernel_launch(void* const* d_in, const int* in_sizes, int n_in, void* d_out, int out_size, void* d_ws, size_t ws_size,
                              hipStream_t stream) {
    const char* x0 = (const char*)d_in[0];
    const char* wqkv = (const char*)d_in[1];
    const char* wo = (const char*)d_in[2];
    const char* wg = (const char*)d_in[4];
    const char* wu = (const char*)d_in[5];
    const char* wd = (const char*)d_in[6];
    char* ws = (char*)d_ws;
    char* out = (char*)d_out;
    k_mega<<<256, 768, 0, stream>>>(x0, wqkv, wo, wg, wu, wd, ws, out);
}
```
